```python
import torch

def _softmax(x, axis=-1):
    return torch.softmax(x, dim=axis)
def _gelu(x):
    return torch.nn.functional.gelu(x, approximate="tanh")
def _silu(x):
    return torch.nn.functional.silu(x)
def _sigmoid(x):
    return torch.sigmoid(x)
def _layernorm(x, axis=-1, eps=1e-5):
    m = x.mean(dim=axis, keepdim=True)
    v = x.var(dim=axis, keepdim=True, unbiased=False)
    return (x - m) / torch.sqrt(v + eps)
def _scatter_add(tgt, idx, src, axis=0):
    return tgt.clone().index_add_(axis, idx, src)
def _segment_sum(src, seg, n):
    out = torch.zeros((n,) + src.shape[1:], dtype=src.dtype)
    return out.index_add_(0, seg, src)
def _pad(x, pw):
    flat = tuple(v for pair in reversed(pw) for v in pair)
    return torch.nn.functional.pad(x, flat)

kernel_name = 'sequential_preLN__mha_block__glu_gated_glu__prompt'

def setup_inputs():

    _w = (lambda a: (lambda t: t.to(torch.bfloat16) if t.is_floating_point() else t)(torch.as_tensor(a)))
    def _A_setup():
        B, S, D, H = 1, 2048, 1024, 32
        return {
            "x": _w((lambda *s: torch.randn(*s, dtype=torch.bfloat16) * 0.05)(B, S, D)),
            "wqkv": _w((lambda *s: torch.randn(*s, dtype=torch.bfloat16) * 0.05)(D, 3 * D)),
            "wo": _w((lambda *s: torch.randn(*s, dtype=torch.bfloat16) * 0.05)(D, D)),
        }
    _A_in = _A_setup()

    def _B_setup():
        B, S, D, F = 1, 2048, 1024, 3584
        return {
            "x": _w((lambda *s: torch.randn(*s, dtype=torch.bfloat16) * 0.05)(B, S, D)),
            "w_gate": _w((lambda *s: torch.randn(*s, dtype=torch.bfloat16) * 0.05)(D, F)),
            "w_up": _w((lambda *s: torch.randn(*s, dtype=torch.bfloat16) * 0.05)(D, F)),
            "w_down": _w((lambda *s: torch.randn(*s, dtype=torch.bfloat16) * 0.05)(F, D)),
        }
    _B_in = _B_setup()

    return {
        "A_x": _A_in["x"],
        "A_wqkv": _A_in["wqkv"],
        "A_wo": _A_in["wo"],
        "B_x": _B_in["x"],
        "B_w_gate": _B_in["w_gate"],
        "B_w_up": _B_in["w_up"],
        "B_w_down": _B_in["w_down"],
    }

def reference(A_x, A_wqkv, A_wo, B_x, B_w_gate, B_w_up, B_w_down):
    def _A_ref(x, wqkv, wo):
        B, S, D = x.shape
        H = 32
        Dh = D // H
        qkv = (lambda x, *d: x.permute(*d))((x @ wqkv).reshape(B, S, 3, H, Dh), 2, 0, 3, 1, 4)
        q, k, v = qkv[0], qkv[1], qkv[2]
        scores = torch.einsum("bhqd,bhkd->bhqk", q, k) * (Dh ** -0.5)
        p = _softmax(scores, axis=-1)
        o = torch.einsum("bhqk,bhkd->bhqd", p, v)
        return (lambda x, *d: x.permute(*d))(o, 0, 2, 1, 3).reshape(B, S, D) @ wo

    def _B_ref(x, w_gate, w_up, w_down):
        g = torch.einsum("bsd,df->bsf", x, w_gate)
        u = torch.einsum("bsd,df->bsf", x, w_up)
        h = _sigmoid(g) * u
        return torch.einsum("bsf,fd->bsd", h, w_down)

    x = A_x
    h = x + _A_ref(_layernorm(x, axis=-1), A_wqkv, A_wo)
    h2 = h + _B_ref(_layernorm(h, axis=-1), B_w_gate, B_w_up, B_w_down)
    return h2
```

```cpp
#include <hip/hip_runtime.h>
#include <hip/hip_bf16.h>
#include <stdint.h>
typedef unsigned short u16;
constexpr int S = 2048, D = 1024, H = 32, DH = 32, F = 3584;
__device__ __forceinline__ float bf2f(u16 v) { return __uint_as_float(((unsigned)v) << 16); }
__device__ __forceinline__ u16 f2bf(float f) { unsigned u = __float_as_uint(f); return (u16)((u + 0x7fffu + ((u >> 16) & 1u)) >> 16); }

__global__ void ln_kernel(const u16* x, u16* y) {
  int row = blockIdx.x; int t = threadIdx.x;
  __shared__ float red[256];
  float v[4]; float s = 0;
  for (int i = 0; i < 4; ++i) { v[i] = bf2f(x[row * D + t + 256 * i]); s += v[i]; }
  red[t] = s; __syncthreads();
  for (int o = 128; o > 0; o >>= 1) { if (t < o) red[t] += red[t + o]; __syncthreads(); }
  float mean = red[0] / D; __syncthreads();
  float q = 0; for (int i = 0; i < 4; ++i) { v[i] -= mean; q += v[i] * v[i]; }
  red[t] = q; __syncthreads();
  for (int o = 128; o > 0; o >>= 1) { if (t < o) red[t] += red[t + o]; __syncthreads(); }
  float rstd = 1.0f / sqrtf(red[0] / D + 1e-5f);
  for (int i = 0; i < 4; ++i) y[row * D + t + 256 * i] = f2bf(v[i] * rstd);
}
__global__ void gemm_kernel(const u16* A, const u16* B, const u16* res, u16* C, int M, int N, int K) {
  __shared__ float As[16][17], Bs[16][17];
  int tx = threadIdx.x, ty = threadIdx.y; int r = blockIdx.y * 16 + ty, c = blockIdx.x * 16 + tx;
  float acc = 0;
  for (int k0 = 0; k0 < K; k0 += 16) {
    As[ty][tx] = bf2f(A[(size_t)r * K + k0 + tx]); Bs[ty][tx] = bf2f(B[(size_t)(k0 + ty) * N + c]);
    __syncthreads();
    for (int k = 0; k < 16; ++k) acc += As[ty][k] * Bs[k][tx];
    __syncthreads();
  }
  if (res) acc = bf2f(f2bf(acc)) + bf2f(res[(size_t)r * N + c]);
  C[(size_t)r * N + c] = f2bf(acc);
}
__global__ void glu_kernel(const u16* g, const u16* u, u16* h, int n) {
  int i = blockIdx.x * blockDim.x + threadIdx.x; if (i >= n) return;
  float gv = bf2f(g[i]); float sg = 1.0f / (1.0f + __expf(-gv)); h[i] = f2bf(bf2f(f2bf(sg)) * bf2f(u[i]));
}
__global__ void attn_kernel(const u16* qkv, u16* o) {
  int h = blockIdx.y; int q = blockIdx.x * 64 + threadIdx.x;
  float qv[DH], acc[DH]; for (int d = 0; d < DH; ++d) { qv[d] = bf2f(qkv[(size_t)q * 3 * D + h * DH + d]); acc[d] = 0; }
  float m = -1e30f, l = 0;
  __shared__ float ks[64][DH], vs[64][DH];
  for (int k0 = 0; k0 < S; k0 += 64) {
    __syncthreads();
    for (int i = threadIdx.x; i < 64 * DH; i += 64) { int kk = i / DH, d = i % DH; ks[kk][d] = bf2f(qkv[(size_t)(k0 + kk) * 3 * D + D + h * DH + d]); vs[kk][d] = bf2f(qkv[(size_t)(k0 + kk) * 3 * D + 2 * D + h * DH + d]); }
    __syncthreads();
    for (int kk = 0; kk < 64; ++kk) { float s = 0; for (int d = 0; d < DH; ++d) s += qv[d] * ks[kk][d]; s *= 0.17677669529663687f;
      float mn = fmaxf(m, s); float a = __expf(m - mn), p = __expf(s - mn); l = l * a + p; for (int d = 0; d < DH; ++d) acc[d] = acc[d] * a + p * vs[kk][d]; m = mn; }
  }
  for (int d = 0; d < DH; ++d) o[(size_t)q * D + h * DH + d] = f2bf(acc[d] / l);
}
extern "C" void kernel_launch(void* const* d_in, const int* in_sizes, int n_in, void* d_out, int out_size, void* d_ws, size_t ws_size, hipStream_t stream) {
  const u16* x = (const u16*)d_in[0]; const u16* wqkv = (const u16*)d_in[1]; const u16* wo = (const u16*)d_in[2];
  const u16* wg = (const u16*)d_in[4]; const u16* wu = (const u16*)d_in[5]; const u16* wd = (const u16*)d_in[6];
  u16* ws = (u16*)d_ws; u16* xn = ws; u16* qkv = xn + S * D; u16* o = qkv + S * 3 * D; u16* h = o + S * D; u16* hn = h + S * D; u16* g = hn + S * D; u16* u = g + S * F; u16* a = u + S * F;
  ln_kernel<<<S, 256, 0, stream>>>(x, xn);
  gemm_kernel<<<dim3(3 * D / 16, S / 16), dim3(16, 16), 0, stream>>>(xn, wqkv, nullptr, qkv, S, 3 * D, D);
  attn_kernel<<<dim3(S / 64, H), 64, 0, stream>>>(qkv, o);
  gemm_kernel<<<dim3(D / 16, S / 16), dim3(16, 16), 0, stream>>>(o, wo, x, h, S, D, D);
  ln_kernel<<<S, 256, 0, stream>>>(h, hn);
  gemm_kernel<<<dim3(F / 16, S / 16), dim3(16, 16), 0, stream>>>(hn, wg, nullptr, g, S, F, D);
  gemm_kernel<<<dim3(F / 16, S / 16), dim3(16, 16), 0, stream>>>(hn, wu, nullptr, u, S, F, D);
  glu_kernel<<<(S * F + 255) / 256, 256, 0, stream>>>(g, u, a, S * F);
  gemm_kernel<<<dim3(D / 16, S / 16), dim3(16, 16), 0, stream>>>(a, wd, h, (u16*)d_out, S, D, F);
}
```

```cpp
#include <hip/hip_runtime.h>
#include <stdint.h>
#include <utility>
#include <type_traits>

typedef unsigned short u16;
typedef unsigned int u32;
typedef unsigned long long u64;
typedef float f32x2 __attribute__((ext_vector_type(2)));
typedef float f32x4 __attribute__((ext_vector_type(4)));
typedef float f32x16 __attribute__((ext_vector_type(16)));
typedef u32 u32x2 __attribute__((ext_vector_type(2)));
typedef u32 u32x4 __attribute__((ext_vector_type(4)));
typedef __bf16 bf16x2 __attribute__((ext_vector_type(2)));
typedef __bf16 bf16x8 __attribute__((ext_vector_type(8)));
typedef __amdgpu_buffer_rsrc_t rsrc_t;
#define LAS __attribute__((address_space(3)))
#define DI __device__ __forceinline__

constexpr int S = 2048, D = 1024, NH = 32, FF = 3584;
constexpr float LN_EPS = 1e-5f;
constexpr float LOG2E = 1.4426950408889634f;
constexpr float QSCALE = 0.17677669529663687f * LOG2E;

constexpr size_t WS_XN = 0;
constexpr size_t WS_Q = WS_XN + (size_t)S * D * 2;
constexpr size_t WS_KV = WS_Q + (size_t)S * D * 2;
constexpr size_t WS_O = WS_KV + (size_t)S * D * 4;
constexpr size_t WS_HC = WS_O + (size_t)S * D * 2;
constexpr size_t WS_ACT = WS_HC + (size_t)S * D * 2;
constexpr size_t WS_ST = WS_ACT + (size_t)S * FF * 2;
constexpr size_t WS_MX = WS_ST + (size_t)S * 16 * 8;
constexpr size_t WS_WB = WS_MX + S * 4;
constexpr size_t WS_FL = WS_WB + 4096;
constexpr size_t WS_END = WS_FL + 256 + 5 * 2048;
constexpr int LDS_MERGE = 131072, LDS_XRES = 131072, LDS_WB = 147456, LDS_MU = 153600, LDS_DUMMY = 159744, LDS_BYTES = 163840;
enum { FL_XN = 0, FL_QKV, FL_ATT, FL_H, FL_ACT };

DI rsrc_t mk_rsrc(const void* p, u32 bytes) { return __builtin_amdgcn_make_buffer_rsrc((void*)p, (short)0, (int)bytes, 0x00020000); }
DI u32 rfl(u32 x) { return __builtin_amdgcn_readfirstlane(x); }
DI u32 cvtpk(float lo, float hi) { f32x2 v = {lo, hi}; bf16x2 b = __builtin_convertvector(v, bf16x2); return __builtin_bit_cast(u32, b); }
DI float bflo(u32 w) { return __uint_as_float(w << 16); }
DI float bfhi(u32 w) { return __uint_as_float(w & 0xffff0000u); }
template <int N> DI void wait_vm() { asm volatile("s_waitcnt vmcnt(%0)" ::"n"(N) : "memory"); }
template <int N> DI void wait_lgkm() { asm volatile("s_waitcnt lgkmcnt(%0)" ::"n"(N) : "memory"); }
DI void bar() { asm volatile("s_barrier" ::: "memory"); }
DI float xsum16(float x) { auto r = __builtin_amdgcn_permlane16_swap(__float_as_uint(x), __float_as_uint(x), false, false); return __uint_as_float(r[0]) + __uint_as_float(r[1]); }
DI float xsum32(float x) { auto r = __builtin_amdgcn_permlane32_swap(__float_as_uint(x), __float_as_uint(x), false, false); return __uint_as_float(r[0]) + __uint_as_float(r[1]); }
DI float gsum(float x) { return xsum32(xsum16(x)); }
template <int OFF> DI void ld128(u32x4& d, u32 voff, rsrc_t srd, u32 so) { asm volatile("buffer_load_dwordx4 %0, %1, %2, %3 offen offset:%4 sc1" : "=v"(d) : "v"(voff), "s"(srd), "s"(so), "i"(OFF)); }
DI void st128(rsrc_t srd, u32 off, const u32x4& v) { __builtin_amdgcn_raw_buffer_store_b128(v, srd, off, 0, 16); }
DI void st64(rsrc_t srd, u32 off, const u32x2& v) { __builtin_amdgcn_raw_buffer_store_b64(v, srd, off, 0, 16); }
#define PINV(x) asm volatile("" : "+v"(x))


struct Ctx {
  const u16 *x, *wqkv, *wo, *wg, *wu, *wd;
  char* ws; u16* out;
  u32 w, ldsbase, xcd, bi;
  u64 tag;
  rsrc_t wsr;
};
DI u32 fresh_lane() { u32 l; asm volatile("v_mbcnt_lo_u32_b32 %0, -1, 0\n\tv_mbcnt_hi_u32_b32 %0, -1, %0" : "=v"(l)); return l; }
DI u64* flag_ptr(const Ctx& c, int arr, u32 i) { return (u64*)(c.ws + WS_FL + 256 + arr * 2048) + i; }
DI void publish(const Ctx& c, int arr, u32 i) {
  asm volatile("s_waitcnt vmcnt(0)\n\ts_barrier" ::: "memory");
  if (c.w == 0 && fresh_lane() == 0) __hip_atomic_store(flag_ptr(c, arr, i), c.tag, __ATOMIC_RELAXED, __HIP_MEMORY_SCOPE_AGENT);
}
DI void await(const Ctx& c, int arr, u32 idx) {
  if (c.w == 0) {
    const u64* p = flag_ptr(c, arr, idx == 0xffffffffu ? 0u : idx);
#pragma unroll 1
    for (u32 spin = 0; spin < (1u << 14); ++spin) {
      const u64 v = __hip_atomic_load(p, __ATOMIC_RELAXED, __HIP_MEMORY_SCOPE_AGENT);
      if (__all(idx == 0xffffffffu || v == c.tag)) break;
      __builtin_amdgcn_s_sleep(2);
    }
  }
  asm volatile("s_barrier" ::: "memory");
}

template <class C> struct Eng {
  f32x4 acc[C::TB][C::NB];
  u32x4 tok[C::RT][C::SUB][C::TB];
  u32x2 wlo[C::FR], whi[C::FR];
  u32 laddr[C::NBASE][C::NBP];
  u32 wvoff[C::NVO];
  u32 tvoff[C::NTV];
  rsrc_t wsrd[C::NSRD];
  rsrc_t tsrd;
  u32 xaddr;
  u32 m0base;
  u32 wsoff, tsoff;
  u32 pfvo[2], pfsink;
  rsrc_t pftsrd;
};
template <class C, int WHICH> DI void issue_pf(Eng<C>& e, int ahead) {
  if constexpr (WHICH == 0) asm volatile("buffer_load_dword %0, %1, %2, %3 offen sc1" : "+v"(e.pfsink) : "v"(e.pfvo[0]), "s"(e.pftsrd), "s"(e.tsoff + ahead * C::SUB * C::TB * 1024));
  else asm volatile("buffer_load_dword %0, %1, %2, %3 offen" : "+v"(e.pfsink) : "v"(e.pfvo[1]), "s"(e.wsrd[0]), "s"(e.wsoff + ahead * C::WSTEP));
}
template <class C, int BUF, int V> DI void issue_m0(Eng<C>& e) {
  asm volatile("s_add_u32 m0, %0, %1" ::"s"(e.m0base), "i"(BUF * C::STAGE + C::dma_lds(V)) : "scc");
}
template <class C, int V> DI void issue_dma(Eng<C>& e, u32 so) {
  asm volatile("buffer_load_dwordx4 %0, %1, %2 offen lds" ::"v"(e.wvoff[C::dma_vo(V)]), "s"(e.wsrd[C::dma_srd(V)]), "s"(so) : "memory");
}
template <class C, int BUF, int T> DI void issue_tok(Eng<C>& e, u32 so) {
  constexpr int sub = T / C::TB, tb = T % C::TB, off = T * 1024;
  if constexpr (C::OPA) asm volatile("buffer_load_dwordx4 %0, %1, %2, %3 offen offset:%4 sc1" : "=a"(e.tok[BUF][sub][tb]) : "v"(e.tvoff[off >> 12]), "s"(e.tsrd), "s"(so), "i"(off & 4095));
  else asm volatile("buffer_load_dwordx4 %0, %1, %2, %3 offen offset:%4 sc1" : "=v"(e.tok[BUF][sub][tb]) : "v"(e.tvoff[off >> 12]), "s"(e.tsrd), "s"(so), "i"(off & 4095));
}
template <class C, int BUF, int I, int FRI, int HALF> DI void read_w(Eng<C>& e) {
  constexpr int sub = I / C::NB, nb = I % C::NB;
  constexpr int bsel = C::NBASE == 2 ? (BUF >= C::R / 2) : 0;
  constexpr int off = (BUF - bsel * (C::R / 2)) * C::STAGE + C::blk_lds(sub, nb) + HALF * 16 * C::PITCH;
  static_assert(off < 65536, "ds offset");
  u32x2& d = HALF ? e.whi[FRI] : e.wlo[FRI];
  if constexpr (C::special(nb)) asm volatile("ds_read_b64 %0, %1 offset:%2" : "=v"(d) : "v"(e.xaddr), "i"((BUF * C::SUB + sub) * 64 + HALF * 8));
  else asm volatile("ds_read_b64_tr_b16 %0, %1 offset:%2" : "=v"(d) : "v"(e.laddr[bsel][C::nbp(nb)]), "i"(off));
}
template <bool OPA, bool SWAP> DI void mfma16(f32x4& acc, const u32x4& w, const u32x4& t) {
  if constexpr (OPA) asm volatile("v_mfma_f32_16x16x32_bf16 %0, %1, %2, %0" : "+v"(acc) : "v"(w), "a"(t));
  else if constexpr (SWAP) asm volatile("v_mfma_f32_16x16x32_bf16 %0, %1, %2, %0" : "+v"(acc) : "v"(t), "v"(w));
  else asm volatile("v_mfma_f32_16x16x32_bf16 %0, %1, %2, %0" : "+v"(acc) : "v"(w), "v"(t));
}
template <class C, int ST, int I, int K> DI void slot(Eng<C>& e, const u32x4& w) {
  constexpr int NBLK = C::SUB * C::NB, NT = C::SUB * C::TB, NV = C::ND + NT + C::NPF, sub = I / C::NB, nb = I % C::NB;
  constexpr int J = I + C::L, st2 = (J >= NBLK) ? (ST + 1) % C::R : ST, j2 = J % NBLK, fr2 = (ST * NBLK + J) % C::FR;
  constexpr int BUF = (ST + C::R - 1) % C::R, TBUF = (ST + C::RT - 1) % C::RT, V = I - C::BARPOS - 1;
  constexpr bool HASV = V >= 0 && V < NV;
  mfma16<C::OPA, C::swap(nb)>(e.acc[K][nb], w, e.tok[ST % C::RT][sub][K]);
  if constexpr (K == 0 && I == C::BARPOS) { wait_vm<(C::RT - 3) * NV>(); if constexpr (C::ND > 0) bar(); }
  if constexpr (K == 0) read_w<C, st2, j2, fr2, 0>(e);
  if constexpr (K == 1) read_w<C, st2, j2, fr2, 1>(e);
  constexpr int KM = C::TB == 4 ? 2 : 0, KV = C::TB - 1;
  if constexpr (K == KM && HASV && V < C::ND) issue_m0<C, BUF, V>(e);
  if constexpr (K == KV && HASV) { if constexpr (V < C::ND) issue_dma<C, V>(e, e.wsoff); else if constexpr (V < C::ND + NT) issue_tok<C, TBUF, V - C::ND>(e, e.tsoff); else issue_pf<C, V - C::ND - NT>(e, C::PFD); }
}
template <class C, int ST, int I, int... Ks> DI void block_(Eng<C>& e, std::integer_sequence<int, Ks...>) {
  constexpr int NBLK = C::SUB * C::NB, fr = (ST * NBLK + I) % C::FR;
  wait_lgkm<2 * (C::L - 1)>();
  const u32x4 w = __builtin_shufflevector(e.wlo[fr], e.whi[fr], 0, 1, 2, 3);
  (slot<C, ST, I, Ks>(e, w), ...);
}
template <class C, int ST, int... Is> DI void stage(Eng<C>& e, std::integer_sequence<int, Is...>) {
  (block_<C, ST, Is>(e, std::make_integer_sequence<int, C::TB>{}), ...);
  e.wsoff += C::WSTEP; e.tsoff += C::SUB * C::TB * 1024;
}
template <class C, int... STs> DI void ring(Eng<C>& e, std::integer_sequence<int, STs...>) {
  (stage<C, STs>(e, std::make_integer_sequence<int, C::SUB * C::NB>{}), ...);
}
template <class C, int BUF, int V> DI void pro_w1(Eng<C>& e) { issue_m0<C, BUF, V>(e); asm volatile("s_nop 0"); issue_dma<C, V>(e, BUF * C::WSTEP); }
template <class C, int... Is> DI void pro_w(Eng<C>& e, std::integer_sequence<int, Is...>) { (pro_w1<C, Is / C::ND, Is % C::ND>(e), ...); }
template <class C, int... Is> DI void pro_t(Eng<C>& e, std::integer_sequence<int, Is...>) { (issue_tok<C, Is / (C::SUB * C::TB), Is % (C::SUB * C::TB)>(e, (Is / (C::SUB * C::TB)) * C::SUB * C::TB * 1024), ...); }
template <class C, int... Is> DI void pro_reads(Eng<C>& e, std::integer_sequence<int, Is...>) { (read_w<C, 0, Is / 2, (Is / 2) % C::FR, Is & 1>(e), ...); }
template <class C> DI void eng_prefetch_w(Eng<C>& e) {
  asm volatile("s_nop 4");
  pro_w<C>(e, std::make_integer_sequence<int, (C::R - 1) * C::ND>{});
  e.wsoff = (C::R - 1) * C::WSTEP;
}
template <class C> DI void eng_prefetch_t(Eng<C>& e) {
  asm volatile("s_nop 4");
  pro_t<C>(e, std::make_integer_sequence<int, (C::RT - 1) * C::SUB * C::TB>{});
  e.tsoff = (C::RT - 1) * C::SUB * C::TB * 1024;
}
template <class C, int EXTRA = 0> DI void eng_open(Eng<C>& e) {
  wait_vm<(C::RT - 3) * C::SUB * C::TB + EXTRA>(); bar();
  pro_reads<C>(e, std::make_integer_sequence<int, 2 * C::L>{});
}
template <class C> DI void eng_loop(Eng<C>& e) {
  constexpr int NV = C::ND + C::SUB * C::TB + C::NPF;
  static_assert((C::RT - 2) * NV <= 63 && (C::ND == 0 || C::R == C::RT) && C::R % C::RT == 0, "rings");
  static_assert(C::BARPOS + 1 + NV <= C::SUB * C::NB && C::BARPOS + C::L <= C::SUB * C::NB, "block budget");
  static_assert((C::R * C::SUB * C::NB) % C::FR == 0 && C::FR > C::L && 2 * (C::L - 1) <= 15, "fragment ring");
  static_assert(C::NSTAGE % C::R == 0, "stage count");
  for (int it = 0; it < C::NSTAGE / C::R; ++it) ring<C>(e, std::make_integer_sequence<int, C::R>{});
  asm volatile("s_waitcnt vmcnt(0) lgkmcnt(0)\n\ts_nop 7\n\ts_nop 7" ::: "memory");
#pragma unroll
  for (int i = 0; i < C::FR; ++i) { PINV(e.wlo[i]); PINV(e.whi[i]); }
#pragma unroll
  for (int r = 0; r < C::RT; ++r)
#pragma unroll
    for (int s = 0; s < C::SUB; ++s)
#pragma unroll
      for (int t = 0; t < C::TB; ++t) { if constexpr (C::OPA) asm volatile("" : "+a"(e.tok[r][s][t])); else PINV(e.tok[r][s][t]); }
  if constexpr (C::NPF > 0) PINV(e.pfsink);
}
template <class C> DI void eng_laddr(Eng<C>& e, u32 ldsbase, u32 lane) {
  const u32 g = lane >> 4, q = (lane >> 2) & 3, p = lane & 3, row = 4 * g + q;
  const u32 f = C::PITCH == 128 ? ((row >> 1) & 3) : (row & 7);
#pragma unroll
  for (int b = 0; b < C::NBASE; ++b)
#pragma unroll
    for (int n = 0; n < C::NBP; ++n) e.laddr[b][n] = ldsbase + b * (C::R / 2) * C::STAGE + row * C::PITCH + ((n ^ f) * 32) + p * 8;
}
template <class C> DI void eng_zero(Eng<C>& e) {
#pragma unroll
  for (int t = 0; t < C::TB; ++t)
#pragma unroll
    for (int n = 0; n < C::NB; ++n) { e.acc[t][n] = (f32x4){0.f, 0.f, 0.f, 0.f}; PINV(e.acc[t][n]); }
}

template <int KTOT> struct CfgOut {
  static constexpr bool OPA = 4 > 4;
  static constexpr int NPF = 0, PFD = 3;
  static constexpr int TB = 2, NB = 4, SUB = 4, R = 4, RT = 4, PITCH = 128, NBP = 4, NBASE = 4 > 4 ? 2 : 1, L = 7, FR = 8, BARPOS = 2, NSTAGE = KTOT / 128;
  static constexpr int ND = 4, NVO = 4, NTV = 2, NSRD = 1, STAGE = 16384, WSTEP = 128 * D * 2;
  static constexpr int dma_lds(int v) { return v * 4096; }
  static constexpr int dma_vo(int v) { return v; }
  static constexpr int dma_srd(int) { return 0; }
  static constexpr int blk_lds(int sub, int) { return sub * 4096; }
  static constexpr int nbp(int nb) { return nb; }
  static constexpr bool swap(int) { return false; }
  static constexpr bool special(int) { return false; }
};
struct CfgWo {
  static constexpr bool OPA = false;
  static constexpr int NPF = 0, PFD = 0;
  static constexpr int TB = 2, NB = 5, SUB = 4, R = 8, RT = 4, PITCH = 128, NBP = 4, NBASE = 2, L = 7, FR = 8, BARPOS = 2, NSTAGE = 8;
  static constexpr int ND = 0, NVO = 1, NTV = 2, NSRD = 1, STAGE = 16384, WSTEP = 0;
  static constexpr int dma_lds(int) { return 0; }
  static constexpr int dma_vo(int) { return 0; }
  static constexpr int dma_srd(int) { return 0; }
  static constexpr int blk_lds(int sub, int) { return sub * 4096; }
  static constexpr int nbp(int nb) { return nb & 3; }
  static constexpr bool swap(int) { return false; }
  static constexpr bool special(int nb) { return nb == 4; }
};

enum { A_NONE = 0, A_TOK = 1  , A_M0 = 16  , A_VM = 32   };
template <class C> struct EngB {
  f32x4 acc[4][C::NBW];
  u32x4 tok[2][4];
  u32x2 wlo[C::FR], whi[C::FR];
  u32 laddr[C::NBASE][C::NBP];
  u32 taddr[C::NBASE];
  u32 vo[C::NVO];
  rsrc_t srd[2];
  u32 m0base[2];
  u32 so[2];
};
template <class C, int BUF, int V> DI void b_m0(EngB<typename C::Sh>& e) { asm volatile("s_add_u32 m0, %0, %1" ::"s"(e.m0base[C::vm_srd(V)]), "i"(BUF * C::STAGE + C::vm_lds(V)) : "scc"); }
template <class C, int V> DI void b_vm(EngB<typename C::Sh>& e, u32 so) {
  if constexpr (C::vm_srd(V) == 0) asm volatile("buffer_load_dwordx4 %0, %1, %2 offen offset:%3 sc1 lds" ::"v"(e.vo[C::vm_vo(V)]), "s"(e.srd[0]), "s"(so), "i"(C::vm_imm(V)) : "memory");
  else asm volatile("buffer_load_dwordx4 %0, %1, %2 offen offset:%3 lds" ::"v"(e.vo[C::vm_vo(V)]), "s"(e.srd[1]), "s"(so), "i"(C::vm_imm(V)) : "memory");
}
template <class C, int BUF> constexpr int b_bsel() { return C::NBASE == 2 ? (BUF >= C::RHALF) : 0; }
template <class C, int BUF, int I, int FRI, int HALF> DI void b_rw(EngB<typename C::Sh>& e) {
  constexpr int u = I / C::NBW, nb = I % C::NBW, bsel = b_bsel<C, BUF>();
  constexpr int off = (BUF - bsel * C::RHALF) * C::STAGE + C::w_lds(u, nb) + HALF * 2048;
  static_assert(off >= 0 && off < 65536, "ds offset");
  u32x2& d = HALF ? e.whi[FRI] : e.wlo[FRI];
  asm volatile("ds_read_b64_tr_b16 %0, %1 offset:%2" : "=v"(d) : "v"(e.laddr[bsel][C::nbp(nb)]), "i"(off));
}
template <class C, int BUF, int U, int SET, int J> DI void b_rt(EngB<typename C::Sh>& e) {
  constexpr int bsel = b_bsel<C, BUF>(), off = (BUF - bsel * C::RHALF) * C::STAGE + C::t_lds(U, J);
  static_assert(off >= 0 && off < 65536, "ds offset");
  asm volatile("ds_read_b128 %0, %1 offset:%2" : "=v"(e.tok[SET][J]) : "v"(e.taddr[bsel]), "i"(off));
}
template <class C> constexpr int b_ldsops(int s) {
  constexpr int NS = C::SUB * C::NBW * 4;
  s = ((s % NS) + NS) % NS;
  int n = (s & 3) < 2 ? 1 : 0;
  for (int i = 0; i < 4; ++i) { const int a = C::act(s, i); if (a >= A_TOK && a < A_M0) ++n; }
  return n;
}
template <class C> constexpr int b_wait(int I) {
  int n = 0;
  for (int s = 4 * (I - C::L) + 1; s < 4 * I; ++s) n += b_ldsops<C>(s);
  n -= 1;
  if (I % C::NBW == 0) {
    int m = 0, s = 4 * I - 1;
    for (;; --s) {
      bool has = false; constexpr int NS = C::SUB * C::NBW * 4;
      for (int i = 0; i < 4; ++i) { const int a = C::act(((s % NS) + NS) % NS, i); if (a >= A_TOK && a < A_M0) has = true; }
      if (has) break;
      m += b_ldsops<C>(s);
    }
    if (m < n) n = m;
  }
  return n;
}
template <class C, int ST, int S_, int AI> DI void b_act(EngB<typename C::Sh>& e) {
  constexpr int a = C::act(S_, AI), I = S_ >> 2, u = I / C::NBW;
  if constexpr (a >= A_TOK && a < A_M0) {
    constexpr int nu = (u + 1) % C::SUB, nbuf = (u + 1 == C::SUB) ? (ST + 1) % C::R : ST;
    b_rt<C, nbuf, nu, (ST * C::SUB + u + 1) & 1, a - A_TOK>(e);
  } else if constexpr (a >= A_M0 && a < A_VM) {
    b_m0<C, (ST + C::R - 1) % C::R, a - A_M0>(e);
  } else if constexpr (a >= A_VM) {
    b_vm<C, a - A_VM>(e, e.so[C::vm_srd(a - A_VM)]);
  }
}
template <class C, int ST, int I, int K> DI void b_slot(EngB<typename C::Sh>& e, const u32x4& w) {
  constexpr int NBLK = C::SUB * C::NBW, NV = C::NV, u = I / C::NBW, nb = I % C::NBW, S_ = 4 * I + K;
  constexpr int J = I + C::L, st2 = (J >= NBLK) ? (ST + 1) % C::R : ST, j2 = J % NBLK, fr2 = (ST * NBLK + J) % C::FR;
  mfma16<false, C::swap(nb)>(e.acc[K][nb], w, e.tok[(ST * C::SUB + u) & 1][K]);
  if constexpr (K == 0 && I == 0) { wait_vm<(C::R - 3) * NV>(); bar(); }
  b_act<C, ST, S_, 0>(e);
  if constexpr (K == 0) b_rw<C, st2, j2, fr2, 0>(e);
  if constexpr (K == 1) b_rw<C, st2, j2, fr2, 1>(e);
  b_act<C, ST, S_, 1>(e);
  if constexpr (C::act(S_, 0) >= A_M0 && C::act(S_, 2) >= A_VM && K >= 2 && C::act(S_, 1) == A_NONE) asm volatile("s_nop 0");
  b_act<C, ST, S_, 2>(e);
  b_act<C, ST, S_, 3>(e);
}
template <class C, int ST, int I> DI void b_block(EngB<typename C::Sh>& e) {
  constexpr int NBLK = C::SUB * C::NBW, fr = (ST * NBLK + I) % C::FR, WT = b_wait<C>(I);
  static_assert(WT >= 0 && WT <= 15, "lgkmcnt range");
  wait_lgkm<WT>();
  const u32x4 w = __builtin_shufflevector(e.wlo[fr], e.whi[fr], 0, 1, 2, 3);
  b_slot<C, ST, I, 0>(e, w); b_slot<C, ST, I, 1>(e, w); b_slot<C, ST, I, 2>(e, w); b_slot<C, ST, I, 3>(e, w);
}
template <class C, int ST, int... Is> DI void b_stage(EngB<typename C::Sh>& e, std::integer_sequence<int, Is...>) {
  (b_block<C, ST, Is>(e), ...);
  e.so[0] += C::TSTEP; e.so[1] += C::WSTEP;
}
template <class C, int... STs> DI void b_ring(EngB<typename C::Sh>& e, std::integer_sequence<int, STs...>) {
  (b_stage<C, STs>(e, std::make_integer_sequence<int, C::SUB * C::NBW>{}), ...);
}
template <class C, int BUF, int V> DI void b_pro1(EngB<typename C::Sh>& e) {
  b_m0<C, BUF, V>(e); asm volatile("s_nop 0");
  b_vm<C, V>(e, BUF * (C::vm_srd(V) ? C::WSTEP : C::TSTEP));
}
template <class C, int... Is> DI void b_pro(EngB<typename C::Sh>& e, std::integer_sequence<int, Is...>) { (b_pro1<C, Is / C::NV, Is % C::NV>(e), ...); }
template <class C, int SRC, int... Is> DI void b_pro_sel(EngB<typename C::Sh>& e, std::integer_sequence<int, Is...>) {
  ([&] { if constexpr (C::vm_srd(Is % C::NV) == SRC) b_pro1<C, Is / C::NV, Is % C::NV>(e); }(), ...);
}
template <class C, int SRC> DI void b_prefetch_sel(EngB<typename C::Sh>& e) {
  asm volatile("s_nop 4");
  b_pro_sel<C, SRC>(e, std::make_integer_sequence<int, (C::R - 1) * C::NV>{});
  e.so[SRC] = (C::R - 1) * (SRC ? C::WSTEP : C::TSTEP);
}
template <class C> DI void b_prefetch(EngB<typename C::Sh>& e) {
  asm volatile("s_nop 4");
  b_pro<C>(e, std::make_integer_sequence<int, (C::R - 1) * C::NV>{});
  e.so[0] = (C::R - 1) * C::TSTEP; e.so[1] = (C::R - 1) * C::WSTEP;
}
template <class C, int... Is> DI void b_first_w(EngB<typename C::Sh>& e, std::integer_sequence<int, Is...>) { (b_rw<C, 0, Is / 2, (Is / 2) % C::FR, Is & 1>(e), ...); }
template <class C, int NWAIT> DI void b_open(EngB<typename C::Sh>& e) {
  wait_vm<NWAIT>(); bar();
  b_rt<C, 0, 0, 0, 0>(e); b_rt<C, 0, 0, 0, 1>(e); b_rt<C, 0, 0, 0, 2>(e); b_rt<C, 0, 0, 0, 3>(e);
  b_first_w<C>(e, std::make_integer_sequence<int, 2 * C::L>{});
  wait_lgkm<0>();
}
template <class C> DI void b_loop(EngB<typename C::Sh>& e) {
  static_assert((C::R - 2) * C::NV <= 63, "vmcnt range");
  static_assert((C::R * C::SUB * C::NBW) % C::FR == 0 && C::FR > C::L && (C::R * C::SUB) % 2 == 0, "rings");
  for (int it = 0; it < C::NSTAGE / C::R; ++it) b_ring<C>(e, std::make_integer_sequence<int, C::R>{});
  if constexpr (C::NSTAGE % C::R != 0) b_ring<C>(e, std::make_integer_sequence<int, C::NSTAGE % C::R>{});
  asm volatile("s_waitcnt vmcnt(0) lgkmcnt(0)\n\ts_nop 7\n\ts_nop 7" ::: "memory");
#pragma unroll
  for (int i = 0; i < C::FR; ++i) { PINV(e.wlo[i]); PINV(e.whi[i]); }
#pragma unroll
  for (int i = 0; i < 4; ++i) { PINV(e.tok[0][i]); PINV(e.tok[1][i]); }
}
struct ShQB { static constexpr int NBW = 6, NBASE = 2, NBP = 2, FR = 6, NVO = 4; };
template <int WV> struct CfgQB : ShQB {
  typedef ShQB Sh;
  static constexpr int SUB = 1, R = 6, RHALF = 6 / 2, L = 3, NSTAGE = 32, NV = 5;
  static constexpr int STAGE = 20480, TSTEP = 2048, WSTEP = 32 * 3 * D * 2;
  static constexpr int vm_lds(int v) { return v < 2 ? 0 : 8192 + (v - 2) * 4096; }
  static constexpr int vm_vo(int v) { return v < 2 ? 0 : v - 1; }
  static constexpr int vm_srd(int v) { return v < 2 ? 0 : 1; }
  static constexpr int vm_imm(int v) { return v == 1 ? 1024 : 0; }
  static constexpr int w_lds(int, int nb) { return 8192 + (nb >> 1) * 4096; }
  static constexpr int t_lds(int, int j) { return j * 1024; }
  static constexpr int nbp(int nb) { return nb & 1; }
  static constexpr bool swap(int nb) { return nb >= 4; }
  static constexpr int act(int s, int i) {
    const int I = s >> 2, K = s & 3;
    if (i == 0) return (K == WV && I < 5 && I != 1) ? A_M0 + I : A_NONE;
    if (i == 1) return (K == 2 && I < 4) ? A_TOK + I : A_NONE;
    if (i == 2) return (K == WV && I < 5) ? A_VM + I : A_NONE;
    return A_NONE;
  }
};

template <int CTRL> DI float dpp_add(float x) { return x + __uint_as_float(__builtin_amdgcn_update_dpp(0u, __float_as_uint(x), CTRL, 0xf, 0xf, true)); }
DI float wave_sum(float v) { return gsum(dpp_add<0x140>(dpp_add<0x141>(dpp_add<0x4E>(dpp_add<0xB1>(v))))); }
DI void p0_ln(const Ctx& c, LAS char* lds, const u32x4 (&v)[2][2], u32 lane) {
  const u32 b = blockIdx.x, w = c.w, tid = w * 64 + lane;
#pragma unroll
  for (int r = 0; r < 2; ++r) {
    float f[16];
#pragma unroll
    for (int hh = 0; hh < 2; ++hh)
#pragma unroll
      for (int e = 0; e < 4; ++e) { f[hh * 8 + 2 * e] = bflo(v[r][hh][e]); f[hh * 8 + 2 * e + 1] = bfhi(v[r][hh][e]); }
    float s = 0.f;
#pragma unroll
    for (int e = 0; e < 16; ++e) s += f[e];
    const float mean = wave_sum(s) * (1.f / D);
    float q = 0.f;
#pragma unroll
    for (int e = 0; e < 16; ++e) { f[e] -= mean; q += f[e] * f[e]; }
    const float rstd = __builtin_amdgcn_rsqf(wave_sum(q) * (1.f / D) + LN_EPS);
    const u32 t = w * 2 + r;
    if (lane == 0) __builtin_amdgcn_raw_buffer_store_b32(__float_as_uint(mean), c.wsr, WS_MX + (b * 8 + t) * 4, 0, 16);
#pragma unroll
    for (int hh = 0; hh < 2; ++hh) {
      const u32 ch = hh * 64 + lane, ks = ch >> 2, hf = (ch >> 1) & 1, gA = 2 * (ch & 1);
      LAS char* p = lds + ks * 528 + gA * 128 + t * 16 + hf * 8;
      *(LAS u32x2*)p = (u32x2){cvtpk(f[hh * 8 + 0] * rstd, f[hh * 8 + 1] * rstd), cvtpk(f[hh * 8 + 2] * rstd, f[hh * 8 + 3] * rstd)};
      *(LAS u32x2*)(p + 128) = (u32x2){cvtpk(f[hh * 8 + 4] * rstd, f[hh * 8 + 5] * rstd), cvtpk(f[hh * 8 + 6] * rstd, f[hh * 8 + 7] * rstd)};
    }
  }
  asm volatile("s_waitcnt lgkmcnt(0)\n\ts_barrier" ::: "memory");
  const u32 tb = b >> 1;
  const u32 dst = WS_XN + (tb >> 1) * 65536 + (tb & 1) * 1024 + (b & 1) * 128;
#pragma unroll
  for (int i = 0; i < 4; ++i) {
    const u32 ch = tid + 256 * i, ks = ch >> 5, g = (ch >> 3) & 3, t8 = ch & 7;
    const u32x4 val = *(LAS u32x4*)(lds + ks * 528 + (ch & 31) * 16);
    st128(c.wsr, dst + ks * 2048 + g * 256 + t8 * 16, val);
  }
}

DI void p1_setup(const Ctx& c, EngB<ShQB>& e, u32 rb, u32 hp, u32 lane) {
  typedef CfgQB<3> C;
  const u32 w = c.w, wm = w >> 1, wn = w & 1;
  { const u32 r = lane >> 3, c16 = lane & 7, nb = (c16 >> 1) ^ ((r >> 1) & 3);
#pragma unroll
    for (int p = 0; p < 3; ++p) e.vo[1 + p] = ((8 * w + r) * (3 * D) + p * D + hp * 64 + nb * 16 + (c16 & 1) * 8) * 2; }
  e.vo[0] = lane * 16;
  e.srd[1] = mk_rsrc(c.wqkv, D * 3 * D * 2);
  e.srd[0] = mk_rsrc(c.ws + WS_XN + (size_t)(rb * 4 + w) * 65536, 65536);
  e.m0base[0] = c.ldsbase + w * 2048; e.m0base[1] = c.ldsbase + w * 1024;
  b_prefetch_sel<C, 1>(e);
  const u32 g = lane >> 4, q = (lane >> 2) & 3, p = lane & 3, row = 4 * g + q, f = (row >> 1) & 3;
#pragma unroll
  for (int b = 0; b < 2; ++b) {
#pragma unroll
    for (int n = 0; n < 2; ++n) e.laddr[b][n] = c.ldsbase + b * C::RHALF * C::STAGE + row * 128 + (((2 * wn + n) ^ f) * 32) + p * 8;
    e.taddr[b] = c.ldsbase + b * C::RHALF * C::STAGE + wm * 4096 + lane * 16;
  }
}
template <int WV> DI void p1_body(const Ctx& c, EngB<ShQB>& e, u32 rb, u32 hp) {
  constexpr u32 wm = WV >> 1, wn = WV & 1;
  b_prefetch_sel<CfgQB<WV>, 0>(e);
#pragma unroll
  for (int t = 0; t < 4; ++t)
#pragma unroll
    for (int n = 0; n < 6; ++n) { e.acc[t][n] = (f32x4){0.f, 0.f, 0.f, 0.f}; PINV(e.acc[t][n]); }
  b_open<CfgQB<WV>, 2>(e);
  b_loop<CfgQB<WV>>(e);
  const u32 lane = fresh_lane(), t = lane & 15, g = lane >> 4, head = hp * 2 + wn;
#pragma unroll
  for (int i = 0; i < 2; ++i) {
    const u32 G = rb * 4 + wm * 2 + i;
    const u32 qd = WS_Q + (head * 64 + G) * 2048, kd = WS_KV + (head * 64 + G) * 4096;
#pragma unroll
    for (int tb = 0; tb < 2; ++tb) {
      const f32x4 a = e.acc[2 * i + tb][0] * QSCALE, b = e.acc[2 * i + tb][1] * QSCALE;
      st128(c.wsr, qd + (g * 32 + tb * 16 + t) * 16, (u32x4){cvtpk(a[0], a[1]), cvtpk(a[2], a[3]), cvtpk(b[0], b[1]), cvtpk(b[2], b[3])});
      const f32x4 cc = e.acc[2 * i + tb][2], d = e.acc[2 * i + tb][3];
      st128(c.wsr, kd + (g * 32 + 16 * (t >> 3) + 8 * tb + (t & 7)) * 16, (u32x4){cvtpk(cc[0], cc[1]), cvtpk(cc[2], cc[3]), cvtpk(d[0], d[1]), cvtpk(d[2], d[3])});
    }
#pragma unroll
    for (int nb = 0; nb < 2; ++nb) {
      const f32x4 a = e.acc[2 * i][4 + nb], b = e.acc[2 * i + 1][4 + nb];
      st128(c.wsr, kd + 2048 + (g * 32 + 16 * (t >> 3) + 8 * nb + (t & 7)) * 16, (u32x4){cvtpk(a[0], a[1]), cvtpk(a[2], a[3]), cvtpk(b[0], b[1]), cvtpk(b[2], b[3])});
    }
  }
}
DI void phase01(Ctx& c, LAS char* lds, u64 nonce) {
  const u32 b = blockIdx.x, lane = fresh_lane(), w = c.w;
  const u32 rb = (c.xcd & 1) * 8 + (c.bi & 7), hp = (c.xcd >> 1) * 4 + (c.bi >> 3);
  u32x4 xv[2][2];
#pragma unroll
  for (int r = 0; r < 2; ++r)
#pragma unroll
    for (int hh = 0; hh < 2; ++hh) xv[r][hh] = *(const u32x4*)(c.x + (size_t)(b * 8 + w * 2 + r) * D + hh * 512 + lane * 8);
  EngB<ShQB> e;
  LAS char* scr = lds + LDS_MERGE;
  p0_ln(c, scr, xv, lane);
  c.tag = ((u64)rfl((u32)(nonce >> 32)) << 32 | rfl((u32)nonce)) + 1;
  p1_setup(c, e, rb, hp, lane);
  asm volatile("s_waitcnt vmcnt(15)\n\ts_barrier" ::: "memory");
  if (w == 0 && lane == 0) __hip_atomic_store(flag_ptr(c, FL_XN, b), c.tag, __ATOMIC_RELAXED, __HIP_MEMORY_SCOPE_AGENT);
  if (w != 0) {
    u32x4 wv[2][2];
    const u32 k0 = b * 4 + w - 1, k1 = b * 4 + 3;
    { const rsrc_t wr = mk_rsrc(c.wo, D * D * 2), wr2 = mk_rsrc(c.wo, w == 1 ? D * D * 2 : 0);
      asm volatile("s_nop 4\n\tbuffer_load_dwordx4 %0, %4, %6, 0 offen\n\tbuffer_load_dwordx4 %1, %4, %6, 0 offen offset:1024\n\t"
                   "buffer_load_dwordx4 %2, %5, %7, 0 offen\n\tbuffer_load_dwordx4 %3, %5, %7, 0 offen offset:1024"
                   : "=&v"(wv[0][0]), "=&v"(wv[0][1]), "=&v"(wv[1][0]), "=&v"(wv[1][1]) : "v"(k0 * D * 2 + lane * 16), "v"(k1 * D * 2 + lane * 16), "s"(wr), "s"(wr2)); }
    { const rsrc_t wr = mk_rsrc(c.wqkv, D * 3 * D * 2);
      const u32 vo = (((c.bi & 7) * 128 + (lane >> 3)) * (3 * D) + (w - 1) * D + hp * 64 + (lane & 7) * 8) * 2;
      asm volatile("s_mov_b32 m0, %0\n\ts_nop 4" ::"s"(c.ldsbase + LDS_DUMMY + w * 1024));
#pragma unroll
      for (int i = 0; i < 16; ++i) asm volatile("buffer_load_dwordx4 %0, %1, %2 offen lds" ::"v"(vo), "s"(wr), "s"(i * 8 * 3 * D * 2) : "memory"); }
    asm volatile("s_waitcnt vmcnt(16)" : "+v"(wv[0][0]), "+v"(wv[0][1]), "+v"(wv[1][0]), "+v"(wv[1][1])::"memory");
#pragma unroll
    for (int i = 0; i < 2; ++i) {
      float s = 0.f;
#pragma unroll
      for (int hh = 0; hh < 2; ++hh)
#pragma unroll
        for (int e2 = 0; e2 < 4; ++e2) s += bflo(wv[i][hh][e2]) + bfhi(wv[i][hh][e2]);
      const float m = wave_sum(s) * (1.f / D);
      const u32 hi = cvtpk(m, 0.f) & 0xffffu, lo = cvtpk(m - bflo(hi), 0.f) & 0xffffu;
      const u32 k = i ? k1 : k0, o = k & 31, pos = (k >> 5) * 32 + ((o >> 2) & 3) * 8 + (o >> 4) * 4 + (o & 3);
      if (lane < 2 && (i == 0 || w == 1)) __builtin_amdgcn_raw_buffer_store_b16((u16)(lane ? lo : hi), c.wsr, WS_WB + lane * 2048 + pos * 2, 0, 16);
    }
  }
  await(c, FL_XN, lane < 16 ? rb * 16 + lane : 0xffffffffu);
  if (w == 0) p1_body<0>(c, e, rb, hp); else if (w == 1) p1_body<1>(c, e, rb, hp); else if (w == 2) p1_body<2>(c, e, rb, hp); else p1_body<3>(c, e, rb, hp);
  publish(c, FL_QKV, hp * 16 + rb);
}

DI f32x16 mfma32(const u32x4& a, const u32x4& b, const f32x16& c) {
  return __builtin_amdgcn_mfma_f32_32x32x16_bf16(__builtin_bit_cast(bf16x8, a), __builtin_bit_cast(bf16x8, b), c, 0, 0, 0);
}
DI f32x4 mfma16b(const u32x4& a, const u32x4& b, const f32x4& c) {
  return __builtin_amdgcn_mfma_f32_16x16x32_bf16(__builtin_bit_cast(bf16x8, a), __builtin_bit_cast(bf16x8, b), c, 0, 0, 0);
}
DI void attn_safe(rsrc_t kv, const u32x4& q0, const u32x4& q1, u32 lane, f32x16& o, float& lsum) {
  const u32 selv = (((lane & 3) == 0 && ((lane >> 4) & 1) == (((lane & 15) >> 2) & 1)) ? 0x3f803f80u : 0u);
  const u32x4 sel = {selv, selv, selv, selv};
  f32x4 lacc = {0.f, 0.f, 0.f, 0.f};
  float m = -3.0e38f;
#pragma unroll
  for (int i = 0; i < 16; ++i) o[i] = 0.f;
  for (int g = 0; g < 64; ++g) {
    const u32 off = g * 4096 + lane * 16;
    const u32x4 k0 = __builtin_amdgcn_raw_buffer_load_b128(kv, off, 0, 16), k1 = __builtin_amdgcn_raw_buffer_load_b128(kv, off + 1024, 0, 16);
    const u32x4 v0 = __builtin_amdgcn_raw_buffer_load_b128(kv, off + 2048, 0, 16), v1 = __builtin_amdgcn_raw_buffer_load_b128(kv, off + 3072, 0, 16);
    f32x16 s = {};
    s = mfma32(k0, q0, s);
    s = mfma32(k1, q1, s);
    float mx = s[0];
#pragma unroll
    for (int i = 1; i < 16; ++i) mx = fmaxf(mx, s[i]);
    mx = fmaxf(mx, __shfl_xor(mx, 32));
    const float mn = fmaxf(m, mx), alpha = __builtin_amdgcn_exp2f(m - mn);
    m = mn;
#pragma unroll
    for (int i = 0; i < 16; ++i) { s[i] = __builtin_amdgcn_exp2f(s[i] - mn); o[i] *= alpha; }
    lacc[0] *= alpha;
    const u32x4 p0 = {cvtpk(s[0], s[1]), cvtpk(s[2], s[3]), cvtpk(s[4], s[5]), cvtpk(s[6], s[7])};
    const u32x4 p1 = {cvtpk(s[8], s[9]), cvtpk(s[10], s[11]), cvtpk(s[12], s[13]), cvtpk(s[14], s[15])};
    o = mfma32(v0, p0, o);
    o = mfma32(v1, p1, o);
    lacc = mfma16b(sel, p0, lacc);
    lacc = mfma16b(sel, p1, lacc);
  }
  lsum = lacc[0];
}
struct At {
  u32x4 q[4][2], k[4][2], v[4][2], sel;
  f32x16 s[2], o[4];
  f32x4 l[4];
  u32 p[2][8];
  u32 voff;
  rsrc_t srd;
};
DI void a_qk0(f32x16& s, const u32x4& k, const u32x4& q) { asm volatile("v_mfma_f32_32x32x16_bf16 %0, %1, %2, 0" : "=v"(s) : "v"(k), "v"(q)); }
DI void a_acc32(f32x16& o, const u32x4& a, const u32x4& b) { asm volatile("v_mfma_f32_32x32x16_bf16 %0, %1, %2, %0" : "+a"(o) : "v"(a), "v"(b)); }
DI void a_acc32v(f32x16& o, const u32x4& a, const u32x4& b) { asm volatile("v_mfma_f32_32x32x16_bf16 %0, %1, %2, %0" : "+v"(o) : "v"(a), "v"(b)); }
DI void a_acc16(f32x4& o, const u32x4& a, const u32x4& b) { asm volatile("v_mfma_f32_16x16x32_bf16 %0, %1, %2, %0" : "+a"(o) : "v"(a), "v"(b)); }
struct Pf { rsrc_t srd; u32 vo, so, sostep, m0, m0step; };
constexpr int at_waitn(int T, bool PF, bool FIRST) {
  if (FIRST) { if (T == 0) return 14; if (T == 1) return 12; if (T == 2 || T == 3) return 11; if (T == 5) return 12; if (T == 7) return 13; }
  return (T & 1) ? (PF ? 14 : 9) : -1;
}
template <int T, bool PF, bool FIRST> DI void at_slot(At& a, u32 so, const Pf& pf) {
  constexpr int X = T & 3, G = T >> 2, XN = (X + 1) & 3, XP = (X + 3) & 3, GN = X == 3 ? G + 1 : G, GP = X == 0 ? G + 3 : G, WN = at_waitn(T, PF, FIRST);
  const u32x4 pp0 = {a.p[(T + 1) & 1][0], a.p[(T + 1) & 1][1], a.p[(T + 1) & 1][2], a.p[(T + 1) & 1][3]};
  const u32x4 pp1 = {a.p[(T + 1) & 1][4], a.p[(T + 1) & 1][5], a.p[(T + 1) & 1][6], a.p[(T + 1) & 1][7]};
  float e[16];
  u32x4 ld;
  const u32 pso = pf.so + (T >> 1) * pf.sostep, pm0 = pf.m0 + (T >> 1) * pf.m0step;
  if constexpr (WN >= 0 && PF && !(X & 1)) {
  asm volatile(
    "s_waitcnt vmcnt(%[wn])\n\t"
    "v_mfma_f32_32x32x16_bf16 %[sn], %[k0], %[q0], 0\n\t"
    "v_exp_f32 %[e0], %[s0]\n\t"
    "v_exp_f32 %[e1], %[s1]\n\t"
    "v_exp_f32 %[e2], %[s2]\n\t"
    "v_mfma_f32_32x32x16_bf16 %[o], %[v0], %[pp0], %[o]\n\t"
    "buffer_load_dwordx4 %[ld], %[vo], %[srd], %[so] offen offset:%[lo] sc1\n\t"
    "v_exp_f32 %[e3], %[s3]\n\t"
    "v_exp_f32 %[e4], %[s4]\n\t"
    "v_exp_f32 %[e5], %[s5]\n\t"
    "v_mfma_f32_32x32x16_bf16 %[sn], %[k1], %[q1], %[sn]\n\t"
    "v_exp_f32 %[e6], %[s6]\n\t"
    "v_exp_f32 %[e7], %[s7]\n\t"
    "v_exp_f32 %[e8], %[s8]\n\t"
    "v_mfma_f32_32x32x16_bf16 %[o], %[v1], %[pp1], %[o]\n\t"
    "v_exp_f32 %[e9], %[s9]\n\t"
    "v_exp_f32 %[e10], %[s10]\n\t"
    "v_exp_f32 %[e11], %[s11]\n\t"
    "v_mfma_f32_16x16x32_bf16 %[l], %[sel], %[pp0], %[l]\n\t"
    "s_mov_b32 m0, %[pm0]\n\t"
    "v_exp_f32 %[e12], %[s12]\n\t"
    "v_cvt_pk_bf16_f32 %[p0], %[e0], %[e1]\n\t"
    "v_cvt_pk_bf16_f32 %[p1], %[e2], %[e3]\n\t"
    "v_mfma_f32_16x16x32_bf16 %[l], %[sel], %[pp1], %[l]\n\t"
    "buffer_load_dwordx4 %[pvo], %[psrd], %[pso] offen lds\n\t"
    "v_exp_f32 %[e13], %[s13]\n\t"
    "v_cvt_pk_bf16_f32 %[p2], %[e4], %[e5]\n\t"
    "v_cvt_pk_bf16_f32 %[p3], %[e6], %[e7]\n\t"
    "v_exp_f32 %[e14], %[s14]\n\t"
    "v_exp_f32 %[e15], %[s15]\n\t"
    "v_cvt_pk_bf16_f32 %[p4], %[e8], %[e9]\n\t"
    "v_cvt_pk_bf16_f32 %[p5], %[e10], %[e11]\n\t"
    "v_cvt_pk_bf16_f32 %[p6], %[e12], %[e13]\n\t"
    "v_cvt_pk_bf16_f32 %[p7], %[e14], %[e15]\n\t"
    : [sn] "=&v"(a.s[(T + 1) & 1]), [o] "+a"(a.o[XP]), [l] "+a"(a.l[XP]), [e0] "=&v"(e[0]), [e1] "=&v"(e[1]), [e2] "=&v"(e[2]), [e3] "=&v"(e[3]), [e4] "=&v"(e[4]), [e5] "=&v"(e[5]), [e6] "=&v"(e[6]), [e7] "=&v"(e[7]), [e8] "=&v"(e[8]), [e9] "=&v"(e[9]), [e10] "=&v"(e[10]), [e11] "=&v"(e[11]), [e12] "=&v"(e[12]), [e13] "=&v"(e[13]), [e14] "=&v"(e[14]), [e15] "=&v"(e[15]), [p0] "=&v"(a.p[T & 1][0]), [p1] "=&v"(a.p[T & 1][1]), [p2] "=&v"(a.p[T & 1][2]), [p3] "=&v"(a.p[T & 1][3]), [p4] "=&v"(a.p[T & 1][4]), [p5] "=&v"(a.p[T & 1][5]), [p6] "=&v"(a.p[T & 1][6]), [p7] "=&v"(a.p[T & 1][7]), [ld] "=&v"(ld)
    : [s0] "v"(a.s[T & 1][0]), [s1] "v"(a.s[T & 1][1]), [s2] "v"(a.s[T & 1][2]), [s3] "v"(a.s[T & 1][3]), [s4] "v"(a.s[T & 1][4]), [s5] "v"(a.s[T & 1][5]), [s6] "v"(a.s[T & 1][6]), [s7] "v"(a.s[T & 1][7]), [s8] "v"(a.s[T & 1][8]), [s9] "v"(a.s[T & 1][9]), [s10] "v"(a.s[T & 1][10]), [s11] "v"(a.s[T & 1][11]), [s12] "v"(a.s[T & 1][12]), [s13] "v"(a.s[T & 1][13]), [s14] "v"(a.s[T & 1][14]), [s15] "v"(a.s[T & 1][15]), [k0] "v"(a.k[GN & 3][0]), [k1] "v"(a.k[GN & 3][1]), [q0] "v"(a.q[XN][0]), [q1] "v"(a.q[XN][1]), [v0] "v"(a.v[GP & 3][0]), [v1] "v"(a.v[GP & 3][1]), [pp0] "v"(pp0), [pp1] "v"(pp1), [sel] "v"(a.sel), [vo] "v"(a.voff), [srd] "s"(a.srd), [so] "s"(so), [lo] "i"(X * 1024), [wn] "i"(WN), [pvo] "v"(pf.vo), [psrd] "s"(pf.srd), [pso] "s"(pso), [pm0] "s"(pm0));
  } else if constexpr (WN >= 0) {
  asm volatile(
    "s_waitcnt vmcnt(%[wn])\n\t"
    "v_mfma_f32_32x32x16_bf16 %[sn], %[k0], %[q0], 0\n\t"
    "v_exp_f32 %[e0], %[s0]\n\t"
    "v_exp_f32 %[e1], %[s1]\n\t"
    "v_exp_f32 %[e2], %[s2]\n\t"
    "v_mfma_f32_32x32x16_bf16 %[o], %[v0], %[pp0], %[o]\n\t"
    "buffer_load_dwordx4 %[ld], %[vo], %[srd], %[so] offen offset:%[lo] sc1\n\t"
    "v_exp_f32 %[e3], %[s3]\n\t"
    "v_exp_f32 %[e4], %[s4]\n\t"
    "v_exp_f32 %[e5], %[s5]\n\t"
    "v_mfma_f32_32x32x16_bf16 %[sn], %[k1], %[q1], %[sn]\n\t"
    "v_exp_f32 %[e6], %[s6]\n\t"
    "v_exp_f32 %[e7], %[s7]\n\t"
    "v_exp_f32 %[e8], %[s8]\n\t"
    "v_mfma_f32_32x32x16_bf16 %[o], %[v1], %[pp1], %[o]\n\t"
    "v_exp_f32 %[e9], %[s9]\n\t"
    "v_exp_f32 %[e10], %[s10]\n\t"
    "v_exp_f32 %[e11], %[s11]\n\t"
    "v_mfma_f32_16x16x32_bf16 %[l], %[sel], %[pp0], %[l]\n\t"
    "v_exp_f32 %[e12], %[s12]\n\t"
    "v_cvt_pk_bf16_f32 %[p0], %[e0], %[e1]\n\t"
    "v_cvt_pk_bf16_f32 %[p1], %[e2], %[e3]\n\t"
    "v_mfma_f32_16x16x32_bf16 %[l], %[sel], %[pp1], %[l]\n\t"
    "v_exp_f32 %[e13], %[s13]\n\t"
    "v_cvt_pk_bf16_f32 %[p2], %[e4], %[e5]\n\t"
    "v_cvt_pk_bf16_f32 %[p3], %[e6], %[e7]\n\t"
    "v_exp_f32 %[e14], %[s14]\n\t"
    "v_exp_f32 %[e15], %[s15]\n\t"
    "v_cvt_pk_bf16_f32 %[p4], %[e8], %[e9]\n\t"
    "v_cvt_pk_bf16_f32 %[p5], %[e10], %[e11]\n\t"
    "v_cvt_pk_bf16_f32 %[p6], %[e12], %[e13]\n\t"
    "v_cvt_pk_bf16_f32 %[p7], %[e14], %[e15]\n\t"
    : [sn] "=&v"(a.s[(T + 1) & 1]), [o] "+a"(a.o[XP]), [l] "+a"(a.l[XP]), [e0] "=&v"(e[0]), [e1] "=&v"(e[1]), [e2] "=&v"(e[2]), [e3] "=&v"(e[3]), [e4] "=&v"(e[4]), [e5] "=&v"(e[5]), [e6] "=&v"(e[6]), [e7] "=&v"(e[7]), [e8] "=&v"(e[8]), [e9] "=&v"(e[9]), [e10] "=&v"(e[10]), [e11] "=&v"(e[11]), [e12] "=&v"(e[12]), [e13] "=&v"(e[13]), [e14] "=&v"(e[14]), [e15] "=&v"(e[15]), [p0] "=&v"(a.p[T & 1][0]), [p1] "=&v"(a.p[T & 1][1]), [p2] "=&v"(a.p[T & 1][2]), [p3] "=&v"(a.p[T & 1][3]), [p4] "=&v"(a.p[T & 1][4]), [p5] "=&v"(a.p[T & 1][5]), [p6] "=&v"(a.p[T & 1][6]), [p7] "=&v"(a.p[T & 1][7]), [ld] "=&v"(ld)
    : [s0] "v"(a.s[T & 1][0]), [s1] "v"(a.s[T & 1][1]), [s2] "v"(a.s[T & 1][2]), [s3] "v"(a.s[T & 1][3]), [s4] "v"(a.s[T & 1][4]), [s5] "v"(a.s[T & 1][5]), [s6] "v"(a.s[T & 1][6]), [s7] "v"(a.s[T & 1][7]), [s8] "v"(a.s[T & 1][8]), [s9] "v"(a.s[T & 1][9]), [s10] "v"(a.s[T & 1][10]), [s11] "v"(a.s[T & 1][11]), [s12] "v"(a.s[T & 1][12]), [s13] "v"(a.s[T & 1][13]), [s14] "v"(a.s[T & 1][14]), [s15] "v"(a.s[T & 1][15]), [k0] "v"(a.k[GN & 3][0]), [k1] "v"(a.k[GN & 3][1]), [q0] "v"(a.q[XN][0]), [q1] "v"(a.q[XN][1]), [v0] "v"(a.v[GP & 3][0]), [v1] "v"(a.v[GP & 3][1]), [pp0] "v"(pp0), [pp1] "v"(pp1), [sel] "v"(a.sel), [vo] "v"(a.voff), [srd] "s"(a.srd), [so] "s"(so), [lo] "i"(X * 1024), [wn] "i"(WN));
  } else if constexpr (PF) {
  asm volatile(
    "v_mfma_f32_32x32x16_bf16 %[sn], %[k0], %[q0], 0\n\t"
    "v_exp_f32 %[e0], %[s0]\n\t"
    "v_exp_f32 %[e1], %[s1]\n\t"
    "v_exp_f32 %[e2], %[s2]\n\t"
    "v_mfma_f32_32x32x16_bf16 %[o], %[v0], %[pp0], %[o]\n\t"
    "buffer_load_dwordx4 %[ld], %[vo], %[srd], %[so] offen offset:%[lo] sc1\n\t"
    "v_exp_f32 %[e3], %[s3]\n\t"
    "v_exp_f32 %[e4], %[s4]\n\t"
    "v_exp_f32 %[e5], %[s5]\n\t"
    "v_mfma_f32_32x32x16_bf16 %[sn], %[k1], %[q1], %[sn]\n\t"
    "v_exp_f32 %[e6], %[s6]\n\t"
    "v_exp_f32 %[e7], %[s7]\n\t"
    "v_exp_f32 %[e8], %[s8]\n\t"
    "v_mfma_f32_32x32x16_bf16 %[o], %[v1], %[pp1], %[o]\n\t"
    "v_exp_f32 %[e9], %[s9]\n\t"
    "v_exp_f32 %[e10], %[s10]\n\t"
    "v_exp_f32 %[e11], %[s11]\n\t"
    "v_mfma_f32_16x16x32_bf16 %[l], %[sel], %[pp0], %[l]\n\t"
    "s_mov_b32 m0, %[pm0]\n\t"
    "v_exp_f32 %[e12], %[s12]\n\t"
    "v_cvt_pk_bf16_f32 %[p0], %[e0], %[e1]\n\t"
    "v_cvt_pk_bf16_f32 %[p1], %[e2], %[e3]\n\t"
    "v_mfma_f32_16x16x32_bf16 %[l], %[sel], %[pp1], %[l]\n\t"
    "buffer_load_dwordx4 %[pvo], %[psrd], %[pso] offen lds\n\t"
    "v_exp_f32 %[e13], %[s13]\n\t"
    "v_cvt_pk_bf16_f32 %[p2], %[e4], %[e5]\n\t"
    "v_cvt_pk_bf16_f32 %[p3], %[e6], %[e7]\n\t"
    "v_exp_f32 %[e14], %[s14]\n\t"
    "v_exp_f32 %[e15], %[s15]\n\t"
    "v_cvt_pk_bf16_f32 %[p4], %[e8], %[e9]\n\t"
    "v_cvt_pk_bf16_f32 %[p5], %[e10], %[e11]\n\t"
    "v_cvt_pk_bf16_f32 %[p6], %[e12], %[e13]\n\t"
    "v_cvt_pk_bf16_f32 %[p7], %[e14], %[e15]\n\t"
    : [sn] "=&v"(a.s[(T + 1) & 1]), [o] "+a"(a.o[XP]), [l] "+a"(a.l[XP]), [e0] "=&v"(e[0]), [e1] "=&v"(e[1]), [e2] "=&v"(e[2]), [e3] "=&v"(e[3]), [e4] "=&v"(e[4]), [e5] "=&v"(e[5]), [e6] "=&v"(e[6]), [e7] "=&v"(e[7]), [e8] "=&v"(e[8]), [e9] "=&v"(e[9]), [e10] "=&v"(e[10]), [e11] "=&v"(e[11]), [e12] "=&v"(e[12]), [e13] "=&v"(e[13]), [e14] "=&v"(e[14]), [e15] "=&v"(e[15]), [p0] "=&v"(a.p[T & 1][0]), [p1] "=&v"(a.p[T & 1][1]), [p2] "=&v"(a.p[T & 1][2]), [p3] "=&v"(a.p[T & 1][3]), [p4] "=&v"(a.p[T & 1][4]), [p5] "=&v"(a.p[T & 1][5]), [p6] "=&v"(a.p[T & 1][6]), [p7] "=&v"(a.p[T & 1][7]), [ld] "=&v"(ld)
    : [s0] "v"(a.s[T & 1][0]), [s1] "v"(a.s[T & 1][1]), [s2] "v"(a.s[T & 1][2]), [s3] "v"(a.s[T & 1][3]), [s4] "v"(a.s[T & 1][4]), [s5] "v"(a.s[T & 1][5]), [s6] "v"(a.s[T & 1][6]), [s7] "v"(a.s[T & 1][7]), [s8] "v"(a.s[T & 1][8]), [s9] "v"(a.s[T & 1][9]), [s10] "v"(a.s[T & 1][10]), [s11] "v"(a.s[T & 1][11]), [s12] "v"(a.s[T & 1][12]), [s13] "v"(a.s[T & 1][13]), [s14] "v"(a.s[T & 1][14]), [s15] "v"(a.s[T & 1][15]), [k0] "v"(a.k[GN & 3][0]), [k1] "v"(a.k[GN & 3][1]), [q0] "v"(a.q[XN][0]), [q1] "v"(a.q[XN][1]), [v0] "v"(a.v[GP & 3][0]), [v1] "v"(a.v[GP & 3][1]), [pp0] "v"(pp0), [pp1] "v"(pp1), [sel] "v"(a.sel), [vo] "v"(a.voff), [srd] "s"(a.srd), [so] "s"(so), [lo] "i"(X * 1024), [pvo] "v"(pf.vo), [psrd] "s"(pf.srd), [pso] "s"(pso), [pm0] "s"(pm0));
  } else {
  asm volatile(
    "v_mfma_f32_32x32x16_bf16 %[sn], %[k0], %[q0], 0\n\t"
    "v_exp_f32 %[e0], %[s0]\n\t"
    "v_exp_f32 %[e1], %[s1]\n\t"
    "v_exp_f32 %[e2], %[s2]\n\t"
    "v_mfma_f32_32x32x16_bf16 %[o], %[v0], %[pp0], %[o]\n\t"
    "buffer_load_dwordx4 %[ld], %[vo], %[srd], %[so] offen offset:%[lo] sc1\n\t"
    "v_exp_f32 %[e3], %[s3]\n\t"
    "v_exp_f32 %[e4], %[s4]\n\t"
    "v_exp_f32 %[e5], %[s5]\n\t"
    "v_mfma_f32_32x32x16_bf16 %[sn], %[k1], %[q1], %[sn]\n\t"
    "v_exp_f32 %[e6], %[s6]\n\t"
    "v_exp_f32 %[e7], %[s7]\n\t"
    "v_exp_f32 %[e8], %[s8]\n\t"
    "v_mfma_f32_32x32x16_bf16 %[o], %[v1], %[pp1], %[o]\n\t"
    "v_exp_f32 %[e9], %[s9]\n\t"
    "v_exp_f32 %[e10], %[s10]\n\t"
    "v_exp_f32 %[e11], %[s11]\n\t"
    "v_mfma_f32_16x16x32_bf16 %[l], %[sel], %[pp0], %[l]\n\t"
    "v_exp_f32 %[e12], %[s12]\n\t"
    "v_cvt_pk_bf16_f32 %[p0], %[e0], %[e1]\n\t"
    "v_cvt_pk_bf16_f32 %[p1], %[e2], %[e3]\n\t"
    "v_mfma_f32_16x16x32_bf16 %[l], %[sel], %[pp1], %[l]\n\t"
    "v_exp_f32 %[e13], %[s13]\n\t"
    "v_cvt_pk_bf16_f32 %[p2], %[e4], %[e5]\n\t"
    "v_cvt_pk_bf16_f32 %[p3], %[e6], %[e7]\n\t"
    "v_exp_f32 %[e14], %[s14]\n\t"
    "v_exp_f32 %[e15], %[s15]\n\t"
    "v_cvt_pk_bf16_f32 %[p4], %[e8], %[e9]\n\t"
    "v_cvt_pk_bf16_f32 %[p5], %[e10], %[e11]\n\t"
    "v_cvt_pk_bf16_f32 %[p6], %[e12], %[e13]\n\t"
    "v_cvt_pk_bf16_f32 %[p7], %[e14], %[e15]\n\t"
    : [sn] "=&v"(a.s[(T + 1) & 1]), [o] "+a"(a.o[XP]), [l] "+a"(a.l[XP]), [e0] "=&v"(e[0]), [e1] "=&v"(e[1]), [e2] "=&v"(e[2]), [e3] "=&v"(e[3]), [e4] "=&v"(e[4]), [e5] "=&v"(e[5]), [e6] "=&v"(e[6]), [e7] "=&v"(e[7]), [e8] "=&v"(e[8]), [e9] "=&v"(e[9]), [e10] "=&v"(e[10]), [e11] "=&v"(e[11]), [e12] "=&v"(e[12]), [e13] "=&v"(e[13]), [e14] "=&v"(e[14]), [e15] "=&v"(e[15]), [p0] "=&v"(a.p[T & 1][0]), [p1] "=&v"(a.p[T & 1][1]), [p2] "=&v"(a.p[T & 1][2]), [p3] "=&v"(a.p[T & 1][3]), [p4] "=&v"(a.p[T & 1][4]), [p5] "=&v"(a.p[T & 1][5]), [p6] "=&v"(a.p[T & 1][6]), [p7] "=&v"(a.p[T & 1][7]), [ld] "=&v"(ld)
    : [s0] "v"(a.s[T & 1][0]), [s1] "v"(a.s[T & 1][1]), [s2] "v"(a.s[T & 1][2]), [s3] "v"(a.s[T & 1][3]), [s4] "v"(a.s[T & 1][4]), [s5] "v"(a.s[T & 1][5]), [s6] "v"(a.s[T & 1][6]), [s7] "v"(a.s[T & 1][7]), [s8] "v"(a.s[T & 1][8]), [s9] "v"(a.s[T & 1][9]), [s10] "v"(a.s[T & 1][10]), [s11] "v"(a.s[T & 1][11]), [s12] "v"(a.s[T & 1][12]), [s13] "v"(a.s[T & 1][13]), [s14] "v"(a.s[T & 1][14]), [s15] "v"(a.s[T & 1][15]), [k0] "v"(a.k[GN & 3][0]), [k1] "v"(a.k[GN & 3][1]), [q0] "v"(a.q[XN][0]), [q1] "v"(a.q[XN][1]), [v0] "v"(a.v[GP & 3][0]), [v1] "v"(a.v[GP & 3][1]), [pp0] "v"(pp0), [pp1] "v"(pp1), [sel] "v"(a.sel), [vo] "v"(a.voff), [srd] "s"(a.srd), [so] "s"(so), [lo] "i"(X * 1024));
  }
  if constexpr (X < 2) a.k[(G + 3) & 3][X] = ld; else a.v[(G + 3) & 3][X - 2] = ld;
}
template <bool PF, bool FIRST, int... Ts> DI void at_slots(At& a, u32 so, const Pf& pf, std::integer_sequence<int, Ts...>) { (at_slot<Ts, PF, FIRST>(a, so + (Ts >> 2) * 4096, pf), ...); }
template <int KEEP> DI void p2_finish(const Ctx& c, At& a, LAS char* l3, const char* kv, u32 G0, u32 head) {
  constexpr int GIVE = 2 - KEEP;
  const u32 lane = fresh_lane(), w = c.w;
  const u32 mine = c.ldsbase + LDS_MERGE + w * 4352 + lane * 16, other = c.ldsbase + LDS_MERGE + (w ^ 1) * 4352 + lane * 16;
  f32x16 m[2]; float ml[2];
#pragma unroll
  for (int b = 0; b < 2; ++b) {
    const f32x16& og = a.o[GIVE + b];
    asm volatile("ds_write_b128 %0, %1\n\tds_write_b128 %0, %2 offset:1024\n\tds_write_b128 %0, %3 offset:2048\n\tds_write_b128 %0, %4 offset:3072"
                 ::"v"(mine), "a"(__builtin_shufflevector(og, og, 0, 1, 2, 3)), "a"(__builtin_shufflevector(og, og, 4, 5, 6, 7)),
                   "a"(__builtin_shufflevector(og, og, 8, 9, 10, 11)), "a"(__builtin_shufflevector(og, og, 12, 13, 14, 15)) : "memory");
    asm volatile("ds_write_b32 %0, %1 offset:4096\n\ts_waitcnt lgkmcnt(0)\n\ts_barrier" ::"v"(mine - lane * 12), "a"(a.l[GIVE + b][0]) : "memory");
    f32x4 t0, t1, t2, t3; float tl;
    asm volatile("ds_read_b128 %0, %5\n\tds_read_b128 %1, %5 offset:1024\n\tds_read_b128 %2, %5 offset:2048\n\tds_read_b128 %3, %5 offset:3072\n\t"
                 "ds_read_b32 %4, %6 offset:4096\n\ts_waitcnt lgkmcnt(0)"
                 : "=&v"(t0), "=&v"(t1), "=&v"(t2), "=&v"(t3), "=&v"(tl) : "v"(other), "v"(other - lane * 12) : "memory");
    if (b == 0) asm volatile("s_barrier" ::: "memory");
    const f32x16& ok_ = a.o[KEEP + b];
#pragma unroll
    for (int j = 0; j < 4; ++j) { m[b][j] = ok_[j] + t0[j]; m[b][4 + j] = ok_[4 + j] + t1[j]; m[b][8 + j] = ok_[8 + j] + t2[j]; m[b][12 + j] = ok_[12 + j] + t3[j]; }
    ml[b] = a.l[KEEP + b][0] + tl;
  }
  bool ok = true;
#pragma unroll
  for (int x = 0; x < 2; ++x) {
    float mx = 0.f;
#pragma unroll
    for (int i = 0; i < 16; i += 2) mx = __builtin_fmaxf(__builtin_fmaxf(mx, __builtin_fabsf(m[x][i])), __builtin_fabsf(m[x][i + 1]));
    float nn = 0.f;
#pragma unroll
    for (int i = 0; i < 16; i += 4) nn += (m[x][i] + m[x][i + 1]) + (m[x][i + 2] + m[x][i + 3]);
    ok = ok && ml[x] >= 8.6736174e-19f && ml[x] <= 1.1529215e18f && mx <= 1.0e37f && nn == nn;
  }
  if (__any(!ok)) {
    const rsrc_t kvall = mk_rsrc(kv, 64 * 4096);
    attn_safe(kvall, a.q[KEEP][0], a.q[KEEP][1], lane, m[0], ml[0]);
    attn_safe(kvall, a.q[KEEP + 1][0], a.q[KEEP + 1][1], lane, m[1], ml[1]);
  }
  const u32 q = lane & 31, hh = lane >> 5;
#pragma unroll
  for (int x = 0; x < 2; ++x) {
    const float inv = __builtin_amdgcn_rcpf(ml[x]);
    const u32 od = WS_O + ((G0 + KEEP + x) * 32 + head) * 2048 + (q >> 4) * 1024 + (hh * 16 + (q & 15)) * 16;
#pragma unroll
    for (int s = 0; s < 2; ++s)
      st128(c.wsr, od + s * 512, (u32x4){cvtpk(m[x][8 * s] * inv, m[x][8 * s + 1] * inv), cvtpk(m[x][8 * s + 2] * inv, m[x][8 * s + 3] * inv),
                                         cvtpk(m[x][8 * s + 4] * inv, m[x][8 * s + 5] * inv), cvtpk(m[x][8 * s + 6] * inv, m[x][8 * s + 7] * inv)});
  }
}
DI void phase2(const Ctx& c, LAS char* l3) {
  const u32 lane = fresh_lane(), w = c.w;
  const u32 head = c.xcd * 4 + (c.bi >> 3), qb = c.bi & 7;
  const u32 G0 = qb * 8 + (w >> 1) * 4, half = w & 1;
  const char* kv = c.ws + WS_KV + (size_t)head * 64 * 4096;
  At a;
  a.srd = mk_rsrc(kv + half * 32 * 4096, 32 * 4096);
  const rsrc_t qsrd = mk_rsrc(c.ws + WS_Q + ((size_t)head * 64 + G0) * 2048, 8192);
  a.voff = lane * 16;
  { const u32 selv = (((lane & 3) == 0 && ((lane >> 4) & 1) == (((lane & 15) >> 2) & 1)) ? 0x3f803f80u : 0u); a.sel = (u32x4){selv, selv, selv, selv}; }
#pragma unroll
  for (int x = 0; x < 4; ++x) {
#pragma unroll
    for (int i = 0; i < 16; ++i) a.o[x][i] = 0.f;
    a.l[x] = (f32x4){0.f, 0.f, 0.f, 0.f};
    asm volatile("" : "+a"(a.o[x]), "+a"(a.l[x]));
  }
#pragma unroll
  for (int i = 0; i < 8; ++i) { a.p[0][i] = 0; a.p[1][i] = 0; }
  a.v[3][0] = (u32x4){0, 0, 0, 0}; a.v[3][1] = a.v[3][0];
  await(c, FL_QKV, lane < 16 ? (head >> 1) * 16 + lane : 0xffffffffu);
  asm volatile("s_nop 4");
  ld128<0>(a.k[0][0], a.voff, a.srd, 0); ld128<1024>(a.k[0][1], a.voff, a.srd, 0);
#pragma unroll
  for (int x = 0; x < 3; ++x) { ld128<0>(a.q[x][0], a.voff, qsrd, x * 2048); ld128<1024>(a.q[x][1], a.voff, qsrd, x * 2048); }
  ld128<2048>(a.v[0][0], a.voff, a.srd, 0); ld128<3072>(a.v[0][1], a.voff, a.srd, 0);
  ld128<0>(a.q[3][0], a.voff, qsrd, 3 * 2048); ld128<1024>(a.q[3][1], a.voff, qsrd, 3 * 2048);
#pragma unroll
  for (int g = 1; g < 3; ++g) {
    ld128<0>(a.k[g][0], a.voff, a.srd, g * 4096); ld128<1024>(a.k[g][1], a.voff, a.srd, g * 4096);
    ld128<2048>(a.v[g][0], a.voff, a.srd, g * 4096); ld128<3072>(a.v[g][1], a.voff, a.srd, g * 4096);
  }
  wait_vm<16>();
  a_qk0(a.s[0], a.k[0][0], a.q[0][0]); a_acc32v(a.s[0], a.k[0][1], a.q[0][1]);
  asm volatile("s_nop 7\n\ts_nop 7");
  { const u32 b = blockIdx.x, cb3 = (c.xcd & 1) * 8 + (c.bi >> 2);
    const u32 voWo = ((8 * w + (lane >> 3)) * D + cb3 * 64 + ((((lane & 7) >> 1) ^ ((lane >> 4) & 3)) * 16) + (lane & 1) * 8) * 2;
    const u32 voGu = ((lane >> 4) * FF + ((b % 56) % 28) * 128 + (lane & 15) * 8) * 2;
    const u32 dummy = c.ldsbase + LDS_DUMMY + w * 1024;
    Pf pf;
    pf.srd = mk_rsrc(c.wo, D * D * 2); pf.vo = voWo; pf.so = 0; pf.sostep = 65536; pf.m0 = c.ldsbase + w * 1024; pf.m0step = 4096;
    at_slots<true, true>(a, 3 * 4096, pf, std::make_integer_sequence<int, 16>{});
#pragma unroll 1
    for (u32 it = 1; it < 7; ++it) {
      if (it < 4) { pf.so = it * 8 * 65536; pf.m0 = c.ldsbase + w * 1024 + it * 8 * 4096; }
      else if (it < 6) { pf.srd = mk_rsrc((b % 56) >= 28 ? c.wu : c.wg, b < 224 ? D * FF * 2 : 0); pf.vo = voGu; pf.so = ((b / 56) * 256 + w * 64 + (it - 4) * 32) * FF * 2; pf.sostep = 4 * FF * 2; pf.m0 = dummy; pf.m0step = 0; }
      else { pf.srd = mk_rsrc(c.wd, FF * D * 2); pf.vo = a.voff; pf.so = b * 28672 + w * 7168; pf.sostep = 1024; pf.m0 = dummy; pf.m0step = 0; }
      at_slots<true, false>(a, (it * 4 + 3) * 4096, pf, std::make_integer_sequence<int, 16>{});
    }
    at_slots<false, false>(a, (7 * 4 + 3) * 4096, pf, std::make_integer_sequence<int, 16>{}); }
  { const u32x4 pp0 = {a.p[1][0], a.p[1][1], a.p[1][2], a.p[1][3]}, pp1 = {a.p[1][4], a.p[1][5], a.p[1][6], a.p[1][7]};
    asm volatile("s_nop 1");
    a_acc32(a.o[3], a.v[3][0], pp0); a_acc32(a.o[3], a.v[3][1], pp1); a_acc16(a.l[3], a.sel, pp0); a_acc16(a.l[3], a.sel, pp1); }
  asm volatile("s_waitcnt vmcnt(0)\n\ts_nop 7\n\ts_nop 7" ::: "memory");
#pragma unroll
  for (int i = 0; i < 4; ++i) { PINV(a.k[i][0]); PINV(a.k[i][1]); PINV(a.v[i][0]); PINV(a.v[i][1]); }
  PINV(a.s[0]); PINV(a.s[1]);
  if (half) p2_finish<2>(c, a, l3, kv, G0, head); else p2_finish<0>(c, a, l3, kv, G0, head);
  publish(c, FL_ATT, qb * 32 + head);
}

template <class C> DI void out_setup(const Ctx& c, Eng<C>& e, const u16* W, const char* tok, u32 tokbytes, u32 cb, u32 lane) {
  const u32 w = c.w, r = lane >> 3, c16 = lane & 7, nb = (c16 >> 1) ^ ((r >> 1) & 3);
#pragma unroll
  for (int j = 0; j < C::SUB; ++j) e.wvoff[j] = ((32 * j + 8 * w + r) * D + cb * 64 + nb * 16 + (c16 & 1) * 8) * 2;
#pragma unroll
  for (int j = 0; j < C::NTV; ++j) e.tvoff[j] = lane * 16 + 4096 * j;
  e.wsrd[0] = mk_rsrc(W, C::NSTAGE * C::SUB * 32 * D * 2);
  e.tsrd = mk_rsrc(tok, tokbytes);
  e.m0base = c.ldsbase + w * 1024;
}
DI void phase3(const Ctx& c, LAS char* lds) {
  typedef CfgWo C;
  const u32 lane = fresh_lane(), w = c.w, rb = (c.xcd >> 1) * 4 + (c.bi & 3), cb = (c.xcd & 1) * 8 + (c.bi >> 2);
  const u32 G = rb * 4 + w;
  Eng<C> e;
  e.tvoff[0] = lane * 16; e.tvoff[1] = lane * 16 + 4096;
  e.tsrd = mk_rsrc(c.ws + WS_O + (size_t)G * 65536, 65536);
  {
    const rsrc_t xs = mk_rsrc(c.x, S * D * 2);
    const u32 r = lane >> 3, vo = ((G * 32 + r) * D + cb * 64 + ((lane & 7) ^ r) * 8) * 2;
    const u32 m0v = c.ldsbase + LDS_XRES + w * 4096;
    asm volatile("s_nop 4");
#pragma unroll
    for (int i = 0; i < 4; ++i)
      asm volatile("s_mov_b32 m0, %0\n\ts_nop 0\n\tbuffer_load_dwordx4 %1, %2, %3 offen lds" ::"s"(m0v + i * 1024), "v"(vo), "s"(xs), "s"(i * 8 * D * 2) : "memory");
    asm volatile("s_mov_b32 m0, %0\n\ts_nop 0\n\tbuffer_load_dwordx4 %1, %2, 0 offen sc1 lds" ::"s"(c.ldsbase + LDS_WB + w * 1024), "v"((u32)WS_WB + w * 1024 + lane * 16), "s"(c.wsr) : "memory");
    *(LAS u64*)(lds + LDS_WB + 4096 + (w * 64 + lane) * 8) = 0; }
  float mx[2];
#pragma unroll
  for (int tb = 0; tb < 2; ++tb) mx[tb] = __uint_as_float(__builtin_amdgcn_raw_buffer_load_b32(c.wsr, WS_MX + (G * 32 + tb * 16 + (lane & 15)) * 4, 0, 16));
  e.xaddr = c.ldsbase + LDS_WB + ((lane & 15) < 2 ? (lane & 15) * 2048 : 4096) + (lane >> 4) * 16;
  eng_laddr<C>(e, c.ldsbase, lane);
  eng_zero<C>(e);
  PINV(mx[0]); PINV(mx[1]);
  asm volatile("s_waitcnt vmcnt(0) lgkmcnt(0)" ::: "memory");
  await(c, FL_ATT, lane < 32 ? (rb >> 1) * 32 + lane : 0xffffffffu);
  eng_prefetch_t<C>(e);
  eng_open<C>(e);
  eng_loop<C>(e);
  const u32 lane2 = fresh_lane(), t = lane2 & 15, g = lane2 >> 4;
  LAS char* xl = lds + LDS_XRES + w * 4096;
#pragma unroll
  for (int tb = 0; tb < 2; ++tb) {
    const float mu = mx[tb] + gsum(e.acc[tb][4][0] + e.acc[tb][4][1]);
    u32 pc[4][2]; float s = 0.f, q = 0.f;
#pragma unroll
    for (int nb = 0; nb < 4; ++nb) {
      const u32x2 r = *(LAS u32x2*)(xl + (16 * tb + t) * 128 + (((2 * nb + (g >> 1)) ^ (t & 7)) * 16) + (g & 1) * 8);
      const f32x4 a = e.acc[tb][nb];
      pc[nb][0] = cvtpk((a[0] + bflo(r[0])) - mu, (a[1] + bfhi(r[0])) - mu);
      pc[nb][1] = cvtpk((a[2] + bflo(r[1])) - mu, (a[3] + bfhi(r[1])) - mu);
      const float h0 = bflo(pc[nb][0]), h1 = bfhi(pc[nb][0]), h2 = bflo(pc[nb][1]), h3 = bfhi(pc[nb][1]);
      s += (h0 + h1) + (h2 + h3);
      q += (h0 * h0 + h1 * h1) + (h2 * h2 + h3 * h3);
    }
    const u32 ho = ((G >> 1) * 32 + cb * 2) * 4096 + ((G & 1) * 2 + tb) * 1024 + lane2 * 16;
    const u32x4 f0 = {pc[0][0], pc[0][1], pc[1][0], pc[1][1]}, f1 = {pc[2][0], pc[2][1], pc[3][0], pc[3][1]};
    st128(c.wsr, WS_HC + ho, f0);
    st128(c.wsr, WS_HC + ho + 4096, f1);
    s = gsum(s); q = gsum(q);
    if (g == 0) st64(c.wsr, WS_ST + (G * 2 + tb) * 2048 + ((cb >> 1) & 1) * 1024 + ((cb >> 2) * 16 + t) * 16 + (cb & 1) * 8, (u32x2){__float_as_uint(s), __float_as_uint(q)});
    LAS char* rl = lds + LDS_XRES + w * 4096 + lane2 * 16;
    *(LAS u32x4*)(rl + tb * 2048) = f0; *(LAS u32x4*)(rl + tb * 2048 + 1024) = f1;
    *(LAS float*)(lds + LDS_MU + w * 512 + tb * 256 + lane2 * 4) = mu;
  }
  publish(c, FL_H, rb * 16 + cb);
}
DI void phase5(const Ctx& c, LAS char* lds) {
  typedef CfgOut<FF> C;
  const u32 lane = fresh_lane(), w = c.w, rb = (c.xcd >> 1) * 4 + (c.bi & 3), cb = (c.xcd & 1) * 8 + (c.bi >> 2);
  const u32 G = rb * 4 + w;
  Eng<C> e;
  out_setup<C>(c, e, c.wd, c.ws + WS_ACT + (size_t)G * (112 * 2048), 112 * 2048, cb, lane);
  eng_prefetch_w<C>(e);
  eng_laddr<C>(e, c.ldsbase, lane);
  eng_zero<C>(e);
  await(c, FL_ACT, lane < 32 ? (rb >> 1) * 32 + lane : 0xffffffffu);
  eng_prefetch_t<C>(e);
  eng_open<C>(e);
  eng_loop<C>(e);
  const u32 lane2 = fresh_lane(), t = lane2 & 15, g = lane2 >> 4;
#pragma unroll
  for (int tb = 0; tb < 2; ++tb) {
    const u32 token = G * 32 + tb * 16 + t, od = (token * D + cb * 64) * 2;
    const rsrc_t osr = mk_rsrc(c.out, S * D * 2);
    const float mu = *(LAS float*)(lds + LDS_MU + c.w * 512 + tb * 256 + lane2 * 4);
#pragma unroll
    for (int pr = 0; pr < 2; ++pr) {
      const f32x4 a = e.acc[tb][2 * pr] + mu, b = e.acc[tb][2 * pr + 1] + mu;
      const u32x4 r = *(LAS u32x4*)(lds + LDS_XRES + c.w * 4096 + tb * 2048 + pr * 1024 + lane2 * 16);
      u32 a0 = cvtpk(a[0] + bflo(r[0]), a[1] + bfhi(r[0])), a1 = cvtpk(a[2] + bflo(r[1]), a[3] + bfhi(r[1]));
      u32 b0 = cvtpk(b[0] + bflo(r[2]), b[1] + bfhi(r[2])), b1 = cvtpk(b[2] + bflo(r[3]), b[3] + bfhi(r[3]));
      auto s0 = __builtin_amdgcn_permlane16_swap(a0, b0, false, false);
      auto s1 = __builtin_amdgcn_permlane16_swap(a1, b1, false, false);
      st128(osr, od + (pr * 32 + (g & 1) * 16 + (g >> 1) * 8) * 2, (u32x4){s0[0], s1[0], s0[1], s1[1]});
    }
  }
}
struct EngG {
  f32x4 acc[4][14];
  u32x4 tok[8][1][4];
  u32x2 wlo[4], whi[4];
  u32 laddr[2][7];
  u32 wvoff[2], tvoff;
  rsrc_t wsrd[2], tsrd;
  u32 m0base, wsoff, tsoff;
};
struct GluSo { u32 first, pair[7], last; };
struct GluSc { float zr[4], ri[4]; };
DI f32x2 lo2(const f32x4& v) { return __builtin_shufflevector(v, v, 0, 1); }
DI f32x2 hi2(const f32x4& v) { return __builtin_shufflevector(v, v, 2, 3); }
DI void st128a(rsrc_t srd, u32 voff, u32 so, const u32x4& v) { __builtin_amdgcn_raw_buffer_store_b128(v, srd, voff, so, 16); }
DI void st64a(rsrc_t srd, u32 voff, u32 so, const u32x2& v) { __builtin_amdgcn_raw_buffer_store_b64(v, srd, voff, so, 16); }
constexpr int G_STAGE = 16384, G_WSTEP = 32 * FF * 2;
template <int BUF, int V> DI void g_m0(EngG& e) { asm volatile("s_add_u32 m0, %0, %1" ::"s"(e.m0base), "i"(BUF * G_STAGE + (V >> 1) * 8192 + (V & 1) * 1024) : "scc"); }
template <int V> DI void g_dma(EngG& e, u32 so) { asm volatile("buffer_load_dwordx4 %0, %1, %2 offen lds" ::"v"(e.wvoff[V & 1]), "s"(e.wsrd[V >> 1]), "s"(so) : "memory"); }
template <int SET, int T> DI void g_tok(EngG& e, u32 so) { asm volatile("buffer_load_dwordx4 %0, %1, %2, %3 offen offset:%4 sc1" : "=a"(e.tok[SET][0][T]) : "v"(e.tvoff), "s"(e.tsrd), "s"(so), "i"(T * 1024)); }
template <int BUF, int NBK, int FRI, int HALF> DI void g_rw(EngG& e) {
  constexpr int bsel = BUF >= 4, off = (BUF - 4 * bsel) * G_STAGE + (NBK & 1) * 8192 + HALF * 4096;
  u32x2& d = HALF ? e.whi[FRI] : e.wlo[FRI];
  asm volatile("ds_read_b64_tr_b16 %0, %1 offset:%2" : "=v"(d) : "v"(e.laddr[bsel][NBK >> 1]), "i"(off));
}
template <int NBK> DI void g_mfma(f32x4& acc, const u32x4& w, const u32x4& t) {
  if constexpr (NBK >= 10) asm volatile("v_mfma_f32_16x16x32_bf16 %0, %1, %2, %0" : "+a"(acc) : "v"(w), "a"(t));
  else asm volatile("v_mfma_f32_16x16x32_bf16 %0, %1, %2, %0" : "+v"(acc) : "v"(w), "a"(t));
}
constexpr int g_wait(int mode, int st) { return mode != 1 ? 40 : st == 1 ? 12 : (st >= 2 && st <= 4) ? 16 : st == 5 ? 12 : st == 0 ? 4 : 40; }
template <int MODE, int ST, int I, int K> DI void g_slot(EngG& e, const u32x4& w) {
  constexpr bool EXTRA = MODE == 1 && ST < 4;
  constexpr int BARPOS = EXTRA ? 1 : 4, J = I + 3, st2 = J >= 14 ? (ST + 1) % 8 : ST, j2 = J % 14, fr2 = (ST * 14 + J) % 4, BUF = (ST + 7) % 8;
  constexpr int V = I - BARPOS - 1, NV = EXTRA ? 12 : 8;
  g_mfma<I>(e.acc[K][I], w, e.tok[ST][0][K]);
  if constexpr (K == 0 && I == BARPOS) { wait_vm<g_wait(MODE, ST)>(); bar(); }
  if constexpr (!(MODE == 2 && J >= 14)) { if constexpr (K == 0) g_rw<st2, j2, fr2, 0>(e); if constexpr (K == 1) g_rw<st2, j2, fr2, 1>(e); }
  if constexpr (V >= 0 && V < NV) {
    if constexpr (K == 2 && V < 4) g_m0<BUF, V>(e);
    if constexpr (K == 3) {
      if constexpr (V < 4) g_dma<V>(e, e.wsoff);
      else if constexpr (EXTRA && V < 8) g_tok<(ST + 3) % 8, V - 4>(e, e.tsoff - 4 * 4096);
      else g_tok<BUF, (V - 4) & 3>(e, e.tsoff);
    }
  }
}
template <int MODE, int ST, int I> DI void g_block(EngG& e) {
  constexpr int fr = (ST * 14 + I) % 4, WT = (MODE == 2 && 13 - I < 2) ? 2 * (13 - I) : 4;
  wait_lgkm<WT>();
  const u32x4 w = __builtin_shufflevector(e.wlo[fr], e.whi[fr], 0, 1, 2, 3);
  g_slot<MODE, ST, I, 0>(e, w); g_slot<MODE, ST, I, 1>(e, w); g_slot<MODE, ST, I, 2>(e, w); g_slot<MODE, ST, I, 3>(e, w);
}
template <int MODE, int ST, int... Is> DI void g_stage(EngG& e, std::integer_sequence<int, Is...>) {
  (g_block<MODE, ST, Is>(e), ...);
  e.wsoff += G_WSTEP; e.tsoff += 4096;
}
template <int MODE, int... STs> DI void g_ring(EngG& e, std::integer_sequence<int, STs...>) { (g_stage<MODE, STs>(e, std::make_integer_sequence<int, 14>{}), ...); }
template <int... Is> DI void g_pro_w(EngG& e, std::integer_sequence<int, Is...>) {
  ([&] { g_m0<Is / 4, Is % 4>(e); asm volatile("s_nop 0"); g_dma<Is % 4>(e, (Is / 4) * G_WSTEP); }(), ...);
}
DI void p4_tail(const Ctx& c, EngG& e, const GluSc& sc, const u32 (&p0)[2], const GluSo& so) {
  float gv[2][4], uv[2][4], tz[2][4], tw[2][4]; u32 pk[2][4][2];
  asm volatile("s_waitcnt lgkmcnt(4)");
  { const u32x4 wf = __builtin_shufflevector(e.wlo[0], e.whi[0], 0, 1, 2, 3);
  asm volatile("v_mfma_f32_16x16x32_bf16 %[o0], %[i0], %[i1], %[o0]\n\t" "ds_read_b64_tr_b16 %[o1], %[i2] offset:40960\n\t" : [o0] "+v"(e.acc[0][0]), [o1] "=&v"(e.wlo[3]) : [i0] "v"(wf), [i1] "a"(e.tok[1][0][0]), [i2] "v"(e.laddr[0][0]));
  asm volatile("v_mfma_f32_16x16x32_bf16 %[o0], %[i0], %[i1], %[o0]\n\t" "ds_read_b64_tr_b16 %[o1], %[i2] offset:45056\n\t" : [o0] "+v"(e.acc[1][0]), [o1] "=&v"(e.whi[3]) : [i0] "v"(wf), [i1] "a"(e.tok[1][0][1]), [i2] "v"(e.laddr[0][0]));
  asm volatile("v_mfma_f32_16x16x32_bf16 %[o0], %[i0], %[i1], %[o0]\n\t" : [o0] "+v"(e.acc[2][0]) : [i0] "v"(wf), [i1] "a"(e.tok[1][0][2]));
  asm volatile("v_mfma_f32_16x16x32_bf16 %[o0], %[i0], %[i1], %[o0]\n\t" : [o0] "+v"(e.acc[3][0]) : [i0] "v"(wf), [i1] "a"(e.tok[1][0][3]));
  }
  asm volatile("s_waitcnt lgkmcnt(4)");
  { const u32x4 wf = __builtin_shufflevector(e.wlo[1], e.whi[1], 0, 1, 2, 3);
  asm volatile("v_mfma_f32_16x16x32_bf16 %[o0], %[i0], %[i1], %[o0]\n\t" "ds_read_b64_tr_b16 %[o1], %[i2] offset:49152\n\t" : [o0] "+v"(e.acc[0][1]), [o1] "=&v"(e.wlo[0]) : [i0] "v"(wf), [i1] "a"(e.tok[1][0][0]), [i2] "v"(e.laddr[0][0]));
  asm volatile("v_mfma_f32_16x16x32_bf16 %[o0], %[i0], %[i1], %[o0]\n\t" "ds_read_b64_tr_b16 %[o1], %[i2] offset:53248\n\t" : [o0] "+v"(e.acc[1][1]), [o1] "=&v"(e.whi[0]) : [i0] "v"(wf), [i1] "a"(e.tok[1][0][1]), [i2] "v"(e.laddr[0][0]));
  asm volatile("v_mfma_f32_16x16x32_bf16 %[o0], %[i0], %[i1], %[o0]\n\t" : [o0] "+v"(e.acc[2][1]) : [i0] "v"(wf), [i1] "a"(e.tok[1][0][2]));
  asm volatile("v_mfma_f32_16x16x32_bf16 %[o0], %[i0], %[i1], %[o0]\n\t" : [o0] "+v"(e.acc[3][1]) : [i0] "v"(wf), [i1] "a"(e.tok[1][0][3]));
  }
  asm volatile("s_waitcnt lgkmcnt(4)");
  { const u32x4 wf = __builtin_shufflevector(e.wlo[2], e.whi[2], 0, 1, 2, 3);
  asm volatile("v_mfma_f32_16x16x32_bf16 %[o0], %[i0], %[i1], %[o0]\n\t" "ds_read_b64_tr_b16 %[o1], %[i2] offset:57344\n\t" : [o0] "+v"(e.acc[0][0]), [o1] "=&v"(e.wlo[1]) : [i0] "v"(wf), [i1] "a"(e.tok[2][0][0]), [i2] "v"(e.laddr[0][0]));
  asm volatile("v_mfma_f32_16x16x32_bf16 %[o0], %[i0], %[i1], %[o0]\n\t" "ds_read_b64_tr_b16 %[o1], %[i2] offset:61440\n\t" : [o0] "+v"(e.acc[1][0]), [o1] "=&v"(e.whi[1]) : [i0] "v"(wf), [i1] "a"(e.tok[2][0][1]), [i2] "v"(e.laddr[0][0]));
  asm volatile("v_mfma_f32_16x16x32_bf16 %[o0], %[i0], %[i1], %[o0]\n\t" : [o0] "+v"(e.acc[2][0]) : [i0] "v"(wf), [i1] "a"(e.tok[2][0][2]));
  asm volatile("v_mfma_f32_16x16x32_bf16 %[o0], %[i0], %[i1], %[o0]\n\t" : [o0] "+v"(e.acc[3][0]) : [i0] "v"(wf), [i1] "a"(e.tok[2][0][3]));
  }
  asm volatile("s_waitcnt lgkmcnt(4)");
  { const u32x4 wf = __builtin_shufflevector(e.wlo[3], e.whi[3], 0, 1, 2, 3);
  asm volatile("v_mfma_f32_16x16x32_bf16 %[o0], %[i0], %[i1], %[o0]\n\t" "ds_read_b64_tr_b16 %[o1], %[i2] offset:0\n\t" : [o0] "+v"(e.acc[0][1]), [o1] "=&v"(e.wlo[2]) : [i0] "v"(wf), [i1] "a"(e.tok[2][0][0]), [i2] "v"(e.laddr[1][0]));
  asm volatile("v_mfma_f32_16x16x32_bf16 %[o0], %[i0], %[i1], %[o0]\n\t" "ds_read_b64_tr_b16 %[o1], %[i2] offset:4096\n\t" : [o0] "+v"(e.acc[1][1]), [o1] "=&v"(e.whi[2]) : [i0] "v"(wf), [i1] "a"(e.tok[2][0][1]), [i2] "v"(e.laddr[1][0]));
  asm volatile("v_mfma_f32_16x16x32_bf16 %[o0], %[i0], %[i1], %[o0]\n\t" : [o0] "+v"(e.acc[2][1]) : [i0] "v"(wf), [i1] "a"(e.tok[2][0][2]));
  asm volatile("v_mfma_f32_16x16x32_bf16 %[o0], %[i0], %[i1], %[o0]\n\t" : [o0] "+v"(e.acc[3][1]) : [i0] "v"(wf), [i1] "a"(e.tok[2][0][3]));
  }
  asm volatile("s_waitcnt lgkmcnt(4)");
  { const u32x4 wf = __builtin_shufflevector(e.wlo[0], e.whi[0], 0, 1, 2, 3);
  asm volatile("v_mfma_f32_16x16x32_bf16 %[o0], %[i0], %[i1], %[o0]\n\t" "ds_read_b64_tr_b16 %[o1], %[i2] offset:8192\n\t" : [o0] "+v"(e.acc[0][0]), [o1] "=&v"(e.wlo[3]) : [i0] "v"(wf), [i1] "a"(e.tok[3][0][0]), [i2] "v"(e.laddr[1][0]));
  asm volatile("v_mfma_f32_16x16x32_bf16 %[o0], %[i0], %[i1], %[o0]\n\t" "ds_read_b64_tr_b16 %[o1], %[i2] offset:12288\n\t" : [o0] "+v"(e.acc[1][0]), [o1] "=&v"(e.whi[3]) : [i0] "v"(wf), [i1] "a"(e.tok[3][0][1]), [i2] "v"(e.laddr[1][0]));
  asm volatile("v_mfma_f32_16x16x32_bf16 %[o0], %[i0], %[i1], %[o0]\n\t" : [o0] "+v"(e.acc[2][0]) : [i0] "v"(wf), [i1] "a"(e.tok[3][0][2]));
  asm volatile("v_mfma_f32_16x16x32_bf16 %[o0], %[i0], %[i1], %[o0]\n\t" : [o0] "+v"(e.acc[3][0]) : [i0] "v"(wf), [i1] "a"(e.tok[3][0][3]));
  }
  asm volatile("s_waitcnt lgkmcnt(4)");
  { const u32x4 wf = __builtin_shufflevector(e.wlo[1], e.whi[1], 0, 1, 2, 3);
  asm volatile("v_mfma_f32_16x16x32_bf16 %[o0], %[i0], %[i1], %[o0]\n\t" "ds_read_b64_tr_b16 %[o1], %[i2] offset:16384\n\t" : [o0] "+v"(e.acc[0][1]), [o1] "=&v"(e.wlo[0]) : [i0] "v"(wf), [i1] "a"(e.tok[3][0][0]), [i2] "v"(e.laddr[1][0]));
  asm volatile("v_mfma_f32_16x16x32_bf16 %[o0], %[i0], %[i1], %[o0]\n\t" "ds_read_b64_tr_b16 %[o1], %[i2] offset:20480\n\t" : [o0] "+v"(e.acc[1][1]), [o1] "=&v"(e.whi[0]) : [i0] "v"(wf), [i1] "a"(e.tok[3][0][1]), [i2] "v"(e.laddr[1][0]));
  asm volatile("v_mfma_f32_16x16x32_bf16 %[o0], %[i0], %[i1], %[o0]\n\t" : [o0] "+v"(e.acc[2][1]) : [i0] "v"(wf), [i1] "a"(e.tok[3][0][2]));
  asm volatile("v_mfma_f32_16x16x32_bf16 %[o0], %[i0], %[i1], %[o0]\n\t" : [o0] "+v"(e.acc[3][1]) : [i0] "v"(wf), [i1] "a"(e.tok[3][0][3]));
  }
  asm volatile("s_waitcnt lgkmcnt(4)");
  { const u32x4 wf = __builtin_shufflevector(e.wlo[2], e.whi[2], 0, 1, 2, 3);
  asm volatile("v_mfma_f32_16x16x32_bf16 %[o0], %[i0], %[i1], %[o0]\n\t" "ds_read_b64_tr_b16 %[o1], %[i2] offset:24576\n\t" : [o0] "+v"(e.acc[0][0]), [o1] "=&v"(e.wlo[1]) : [i0] "v"(wf), [i1] "a"(e.tok[4][0][0]), [i2] "v"(e.laddr[1][0]));
  asm volatile("v_mfma_f32_16x16x32_bf16 %[o0], %[i0], %[i1], %[o0]\n\t" "ds_read_b64_tr_b16 %[o1], %[i2] offset:28672\n\t" : [o0] "+v"(e.acc[1][0]), [o1] "=&v"(e.whi[1]) : [i0] "v"(wf), [i1] "a"(e.tok[4][0][1]), [i2] "v"(e.laddr[1][0]));
  asm volatile("v_mfma_f32_16x16x32_bf16 %[o0], %[i0], %[i1], %[o0]\n\t" : [o0] "+v"(e.acc[2][0]) : [i0] "v"(wf), [i1] "a"(e.tok[4][0][2]));
  asm volatile("v_mfma_f32_16x16x32_bf16 %[o0], %[i0], %[i1], %[o0]\n\t" : [o0] "+v"(e.acc[3][0]) : [i0] "v"(wf), [i1] "a"(e.tok[4][0][3]));
  }
  asm volatile("s_waitcnt lgkmcnt(4)");
  { const u32x4 wf = __builtin_shufflevector(e.wlo[3], e.whi[3], 0, 1, 2, 3);
  asm volatile("v_mfma_f32_16x16x32_bf16 %[o0], %[i0], %[i1], %[o0]\n\t" "ds_read_b64_tr_b16 %[o1], %[i2] offset:32768\n\t" : [o0] "+v"(e.acc[0][1]), [o1] "=&v"(e.wlo[2]) : [i0] "v"(wf), [i1] "a"(e.tok[4][0][0]), [i2] "v"(e.laddr[1][0]));
  asm volatile("v_mfma_f32_16x16x32_bf16 %[o0], %[i0], %[i1], %[o0]\n\t" "ds_read_b64_tr_b16 %[o1], %[i2] offset:36864\n\t" : [o0] "+v"(e.acc[1][1]), [o1] "=&v"(e.whi[2]) : [i0] "v"(wf), [i1] "a"(e.tok[4][0][1]), [i2] "v"(e.laddr[1][0]));
  asm volatile("v_mfma_f32_16x16x32_bf16 %[o0], %[i0], %[i1], %[o0]\n\t" : [o0] "+v"(e.acc[2][1]) : [i0] "v"(wf), [i1] "a"(e.tok[4][0][2]));
  asm volatile("v_mfma_f32_16x16x32_bf16 %[o0], %[i0], %[i1], %[o0]\n\t" : [o0] "+v"(e.acc[3][1]) : [i0] "v"(wf), [i1] "a"(e.tok[4][0][3]));
  }
  asm volatile("s_waitcnt lgkmcnt(4)");
  { const u32x4 wf = __builtin_shufflevector(e.wlo[0], e.whi[0], 0, 1, 2, 3);
  asm volatile("v_mfma_f32_16x16x32_bf16 %[o0], %[i0], %[i1], %[o0]\n\t" "ds_read_b64_tr_b16 %[o1], %[i2] offset:40960\n\t" : [o0] "+v"(e.acc[0][0]), [o1] "=&v"(e.wlo[3]) : [i0] "v"(wf), [i1] "a"(e.tok[5][0][0]), [i2] "v"(e.laddr[1][0]));
  asm volatile("v_mfma_f32_16x16x32_bf16 %[o0], %[i0], %[i1], %[o0]\n\t" "ds_read_b64_tr_b16 %[o1], %[i2] offset:45056\n\t" : [o0] "+v"(e.acc[1][0]), [o1] "=&v"(e.whi[3]) : [i0] "v"(wf), [i1] "a"(e.tok[5][0][1]), [i2] "v"(e.laddr[1][0]));
  asm volatile("v_mfma_f32_16x16x32_bf16 %[o0], %[i0], %[i1], %[o0]\n\t" : [o0] "+v"(e.acc[2][0]) : [i0] "v"(wf), [i1] "a"(e.tok[5][0][2]));
  asm volatile("v_mfma_f32_16x16x32_bf16 %[o0], %[i0], %[i1], %[o0]\n\t" : [o0] "+v"(e.acc[3][0]) : [i0] "v"(wf), [i1] "a"(e.tok[5][0][3]));
  }
  asm volatile("s_waitcnt vmcnt(0)\n\ts_barrier" ::: "memory");
  asm volatile("s_waitcnt lgkmcnt(4)");
  { const u32x4 wf = __builtin_shufflevector(e.wlo[1], e.whi[1], 0, 1, 2, 3);
  asm volatile("v_mfma_f32_16x16x32_bf16 %[o0], %[i0], %[i1], %[o0]\n\t" "ds_read_b64_tr_b16 %[o1], %[i2] offset:49152\n\t" : [o0] "+v"(e.acc[0][1]), [o1] "=&v"(e.wlo[0]) : [i0] "v"(wf), [i1] "a"(e.tok[5][0][0]), [i2] "v"(e.laddr[1][0]));
  asm volatile("v_mfma_f32_16x16x32_bf16 %[o0], %[i0], %[i1], %[o0]\n\t" "ds_read_b64_tr_b16 %[o1], %[i2] offset:53248\n\t" : [o0] "+v"(e.acc[1][1]), [o1] "=&v"(e.whi[0]) : [i0] "v"(wf), [i1] "a"(e.tok[5][0][1]), [i2] "v"(e.laddr[1][0]));
  asm volatile("v_mfma_f32_16x16x32_bf16 %[o0], %[i0], %[i1], %[o0]\n\t" : [o0] "+v"(e.acc[2][1]) : [i0] "v"(wf), [i1] "a"(e.tok[5][0][2]));
  asm volatile("v_mfma_f32_16x16x32_bf16 %[o0], %[i0], %[i1], %[o0]\n\t" : [o0] "+v"(e.acc[3][1]) : [i0] "v"(wf), [i1] "a"(e.tok[5][0][3]));
  }
  asm volatile("s_waitcnt lgkmcnt(4)");
  { const u32x4 wf = __builtin_shufflevector(e.wlo[2], e.whi[2], 0, 1, 2, 3);
  asm volatile("v_mfma_f32_16x16x32_bf16 %[o0], %[i0], %[i1], %[o0]\n\t" "ds_read_b64_tr_b16 %[o1], %[i2] offset:57344\n\t" : [o0] "+v"(e.acc[0][0]), [o1] "=&v"(e.wlo[1]) : [i0] "v"(wf), [i1] "a"(e.tok[6][0][0]), [i2] "v"(e.laddr[1][0]));
  asm volatile("v_mfma_f32_16x16x32_bf16 %[o0], %[i0], %[i1], %[o0]\n\t" "ds_read_b64_tr_b16 %[o1], %[i2] offset:61440\n\t" : [o0] "+v"(e.acc[1][0]), [o1] "=&v"(e.whi[1]) : [i0] "v"(wf), [i1] "a"(e.tok[6][0][1]), [i2] "v"(e.laddr[1][0]));
  asm volatile("v_mfma_f32_16x16x32_bf16 %[o0], %[i0], %[i1], %[o0]\n\t" : [o0] "+v"(e.acc[2][0]) : [i0] "v"(wf), [i1] "a"(e.tok[6][0][2]));
  asm volatile("v_mfma_f32_16x16x32_bf16 %[o0], %[i0], %[i1], %[o0]\n\t" : [o0] "+v"(e.acc[3][0]) : [i0] "v"(wf), [i1] "a"(e.tok[6][0][3]));
  }
  asm volatile("s_waitcnt lgkmcnt(4)");
  { const u32x4 wf = __builtin_shufflevector(e.wlo[3], e.whi[3], 0, 1, 2, 3);
  asm volatile("v_mfma_f32_16x16x32_bf16 %[o0], %[i0], %[i1], %[o0]\n\t" "ds_read_b64_tr_b16 %[o1], %[i2] offset:16384\n\t" : [o0] "+v"(e.acc[0][1]), [o1] "=&v"(e.wlo[2]) : [i0] "v"(wf), [i1] "a"(e.tok[6][0][0]), [i2] "v"(e.laddr[0][1]));
  asm volatile("v_mfma_f32_16x16x32_bf16 %[o0], %[i0], %[i1], %[o0]\n\t" "ds_read_b64_tr_b16 %[o1], %[i2] offset:20480\n\t" : [o0] "+v"(e.acc[1][1]), [o1] "=&v"(e.whi[2]) : [i0] "v"(wf), [i1] "a"(e.tok[6][0][1]), [i2] "v"(e.laddr[0][1]));
  asm volatile("v_mfma_f32_16x16x32_bf16 %[o0], %[i0], %[i1], %[o0]\n\t" : [o0] "+v"(e.acc[2][1]) : [i0] "v"(wf), [i1] "a"(e.tok[6][0][2]));
  asm volatile("v_mfma_f32_16x16x32_bf16 %[o0], %[i0], %[i1], %[o0]\n\t" : [o0] "+v"(e.acc[3][1]) : [i0] "v"(wf), [i1] "a"(e.tok[6][0][3]));
  }
  asm volatile("s_waitcnt lgkmcnt(4)");
  { const u32x4 wf = __builtin_shufflevector(e.wlo[0], e.whi[0], 0, 1, 2, 3);
  asm volatile("v_mfma_f32_16x16x32_bf16 %[o0], %[i0], %[i1], %[o0]\n\t" "ds_read_b64_tr_b16 %[o1], %[i2] offset:24576\n\t" : [o0] "+v"(e.acc[0][0]), [o1] "=&v"(e.wlo[3]) : [i0] "v"(wf), [i1] "a"(e.tok[7][0][0]), [i2] "v"(e.laddr[0][1]));
  asm volatile("v_mfma_f32_16x16x32_bf16 %[o0], %[i0], %[i1], %[o0]\n\t" "ds_read_b64_tr_b16 %[o1], %[i2] offset:28672\n\t" : [o0] "+v"(e.acc[1][0]), [o1] "=&v"(e.whi[3]) : [i0] "v"(wf), [i1] "a"(e.tok[7][0][1]), [i2] "v"(e.laddr[0][1]));
  asm volatile("v_mfma_f32_16x16x32_bf16 %[o0], %[i0], %[i1], %[o0]\n\t" : [o0] "+v"(e.acc[2][0]) : [i0] "v"(wf), [i1] "a"(e.tok[7][0][2]));
  asm volatile("v_mfma_f32_16x16x32_bf16 %[o0], %[i0], %[i1], %[o0]\n\t" : [o0] "+v"(e.acc[3][0]) : [i0] "v"(wf), [i1] "a"(e.tok[7][0][3]));
  }
  asm volatile("s_waitcnt lgkmcnt(4)");
  { const u32x4 wf = __builtin_shufflevector(e.wlo[1], e.whi[1], 0, 1, 2, 3);
  asm volatile("v_mfma_f32_16x16x32_bf16 %[o0], %[i0], %[i1], %[o0]\n\t" "ds_read_b64_tr_b16 %[o1], %[i2] offset:32768\n\t" : [o0] "+v"(e.acc[0][1]), [o1] "=&v"(e.wlo[0]) : [i0] "v"(wf), [i1] "a"(e.tok[7][0][0]), [i2] "v"(e.laddr[0][1]));
  asm volatile("v_mfma_f32_16x16x32_bf16 %[o0], %[i0], %[i1], %[o0]\n\t" "ds_read_b64_tr_b16 %[o1], %[i2] offset:36864\n\t" : [o0] "+v"(e.acc[1][1]), [o1] "=&v"(e.whi[0]) : [i0] "v"(wf), [i1] "a"(e.tok[7][0][1]), [i2] "v"(e.laddr[0][1]));
  asm volatile("v_mfma_f32_16x16x32_bf16 %[o0], %[i0], %[i1], %[o0]\n\t" : [o0] "+v"(e.acc[2][1]) : [i0] "v"(wf), [i1] "a"(e.tok[7][0][2]));
  asm volatile("v_mfma_f32_16x16x32_bf16 %[o0], %[i0], %[i1], %[o0]\n\t" : [o0] "+v"(e.acc[3][1]) : [i0] "v"(wf), [i1] "a"(e.tok[7][0][3]));
  }
  asm volatile("s_waitcnt lgkmcnt(4)");
  { const u32x4 wf = __builtin_shufflevector(e.wlo[2], e.whi[2], 0, 1, 2, 3);
  asm volatile("v_mfma_f32_16x16x32_bf16 %[o0], %[i0], %[i1], %[o0]\n\t" "ds_read_b64_tr_b16 %[o1], %[i2] offset:40960\n\t" "v_mul_f32 %[o2], %[i3], %[i4]\n\t" : [o0] "+v"(e.acc[0][2]), [o1] "=&v"(e.wlo[1]), [o2] "=&v"(tz[0][0]) : [i0] "v"(wf), [i1] "a"(e.tok[1][0][0]), [i2] "v"(e.laddr[0][1]), [i3] "v"(e.acc[0][0][0]), [i4] "v"(sc.zr[0]));
  asm volatile("v_mfma_f32_16x16x32_bf16 %[o0], %[i0], %[i1], %[o0]\n\t" "ds_read_b64_tr_b16 %[o1], %[i2] offset:45056\n\t" "v_mul_f32 %[o2], %[i3], %[i4]\n\t" : [o0] "+v"(e.acc[1][2]), [o1] "=&v"(e.whi[1]), [o2] "=&v"(tz[1][0]) : [i0] "v"(wf), [i1] "a"(e.tok[1][0][1]), [i2] "v"(e.laddr[0][1]), [i3] "v"(e.acc[1][0][0]), [i4] "v"(sc.zr[1]));
  asm volatile("v_mfma_f32_16x16x32_bf16 %[o0], %[i0], %[i1], %[o0]\n\t" "v_mul_f32 %[o1], %[i2], %[i3]\n\t" "v_mul_f32 %[o2], %[i4], %[i5]\n\t" "v_mul_f32 %[o3], %[i6], %[i7]\n\t" : [o0] "+v"(e.acc[2][2]), [o1] "=&v"(tz[0][1]), [o2] "=&v"(tz[1][1]), [o3] "=&v"(tz[0][2]) : [i0] "v"(wf), [i1] "a"(e.tok[1][0][2]), [i2] "v"(e.acc[0][0][1]), [i3] "v"(sc.zr[0]), [i4] "v"(e.acc[1][0][1]), [i5] "v"(sc.zr[1]), [i6] "v"(e.acc[0][0][2]), [i7] "v"(sc.zr[0]));
  asm volatile("v_mfma_f32_16x16x32_bf16 %[o0], %[i0], %[i1], %[o0]\n\t" "v_mul_f32 %[o1], %[i2], %[i3]\n\t" "v_mul_f32 %[o2], %[i4], %[i5]\n\t" : [o0] "+v"(e.acc[3][2]), [o1] "=&v"(tz[1][2]), [o2] "=&v"(tz[0][3]) : [i0] "v"(wf), [i1] "a"(e.tok[1][0][3]), [i2] "v"(e.acc[1][0][2]), [i3] "v"(sc.zr[1]), [i4] "v"(e.acc[0][0][3]), [i5] "v"(sc.zr[0]));
  }
  asm volatile("s_waitcnt lgkmcnt(4)");
  { const u32x4 wf = __builtin_shufflevector(e.wlo[3], e.whi[3], 0, 1, 2, 3);
  asm volatile("v_mfma_f32_16x16x32_bf16 %[o0], %[i0], %[i1], %[o0]\n\t" "ds_read_b64_tr_b16 %[o1], %[i2] offset:49152\n\t" "v_mul_f32 %[o2], %[i3], %[i4]\n\t" : [o0] "+v"(e.acc[0][3]), [o1] "=&v"(e.wlo[2]), [o2] "=&v"(tz[1][3]) : [i0] "v"(wf), [i1] "a"(e.tok[1][0][0]), [i2] "v"(e.laddr[0][1]), [i3] "v"(e.acc[1][0][3]), [i4] "v"(sc.zr[1]));
  asm volatile("v_mfma_f32_16x16x32_bf16 %[o0], %[i0], %[i1], %[o0]\n\t" "ds_read_b64_tr_b16 %[o1], %[i2] offset:53248\n\t" "v_exp_f32 %[o2], %[o2]\n\t" : [o0] "+v"(e.acc[1][3]), [o1] "=&v"(e.whi[2]), [o2] "+v"(tz[0][0]) : [i0] "v"(wf), [i1] "a"(e.tok[1][0][1]), [i2] "v"(e.laddr[0][1]));
  asm volatile("v_mfma_f32_16x16x32_bf16 %[o0], %[i0], %[i1], %[o0]\n\t" "v_exp_f32 %[o1], %[o1]\n\t" "v_exp_f32 %[o2], %[o2]\n\t" "v_exp_f32 %[o3], %[o3]\n\t" : [o0] "+v"(e.acc[2][3]), [o1] "+v"(tz[1][0]), [o2] "+v"(tz[0][1]), [o3] "+v"(tz[1][1]) : [i0] "v"(wf), [i1] "a"(e.tok[1][0][2]));
  asm volatile("v_mfma_f32_16x16x32_bf16 %[o0], %[i0], %[i1], %[o0]\n\t" "v_exp_f32 %[o1], %[o1]\n\t" "v_exp_f32 %[o2], %[o2]\n\t" : [o0] "+v"(e.acc[3][3]), [o1] "+v"(tz[0][2]), [o2] "+v"(tz[1][2]) : [i0] "v"(wf), [i1] "a"(e.tok[1][0][3]));
  }
  asm volatile("s_waitcnt lgkmcnt(4)");
  { const u32x4 wf = __builtin_shufflevector(e.wlo[0], e.whi[0], 0, 1, 2, 3);
  asm volatile("v_mfma_f32_16x16x32_bf16 %[o0], %[i0], %[i1], %[o0]\n\t" "ds_read_b64_tr_b16 %[o1], %[i2] offset:57344\n\t" "v_exp_f32 %[o2], %[o2]\n\t" : [o0] "+v"(e.acc[0][2]), [o1] "=&v"(e.wlo[3]), [o2] "+v"(tz[0][3]) : [i0] "v"(wf), [i1] "a"(e.tok[2][0][0]), [i2] "v"(e.laddr[0][1]));
  asm volatile("v_mfma_f32_16x16x32_bf16 %[o0], %[i0], %[i1], %[o0]\n\t" "ds_read_b64_tr_b16 %[o1], %[i2] offset:61440\n\t" "v_exp_f32 %[o2], %[o2]\n\t" : [o0] "+v"(e.acc[1][2]), [o1] "=&v"(e.whi[3]), [o2] "+v"(tz[1][3]) : [i0] "v"(wf), [i1] "a"(e.tok[2][0][1]), [i2] "v"(e.laddr[0][1]));
  asm volatile("v_mfma_f32_16x16x32_bf16 %[o0], %[i0], %[i1], %[o0]\n\t" "v_fma_f32 %[o1], %[o1], %[i2], %[i2]\n\t" "v_fma_f32 %[o2], %[o2], %[i3], %[i3]\n\t" "v_fma_f32 %[o3], %[o3], %[i4], %[i4]\n\t" : [o0] "+v"(e.acc[2][2]), [o1] "+v"(tz[0][0]), [o2] "+v"(tz[1][0]), [o3] "+v"(tz[0][1]) : [i0] "v"(wf), [i1] "a"(e.tok[2][0][2]), [i2] "v"(sc.ri[0]), [i3] "v"(sc.ri[1]), [i4] "v"(sc.ri[0]));
  asm volatile("v_mfma_f32_16x16x32_bf16 %[o0], %[i0], %[i1], %[o0]\n\t" "v_fma_f32 %[o1], %[o1], %[i2], %[i2]\n\t" "v_fma_f32 %[o2], %[o2], %[i3], %[i3]\n\t" : [o0] "+v"(e.acc[3][2]), [o1] "+v"(tz[1][1]), [o2] "+v"(tz[0][2]) : [i0] "v"(wf), [i1] "a"(e.tok[2][0][3]), [i2] "v"(sc.ri[1]), [i3] "v"(sc.ri[0]));
  }
  asm volatile("s_waitcnt lgkmcnt(4)");
  { const u32x4 wf = __builtin_shufflevector(e.wlo[1], e.whi[1], 0, 1, 2, 3);
  asm volatile("v_mfma_f32_16x16x32_bf16 %[o0], %[i0], %[i1], %[o0]\n\t" "ds_read_b64_tr_b16 %[o1], %[i2] offset:0\n\t" "v_fma_f32 %[o2], %[o2], %[i3], %[i3]\n\t" : [o0] "+v"(e.acc[0][3]), [o1] "=&v"(e.wlo[0]), [o2] "+v"(tz[1][2]) : [i0] "v"(wf), [i1] "a"(e.tok[2][0][0]), [i2] "v"(e.laddr[1][1]), [i3] "v"(sc.ri[1]));
  asm volatile("v_mfma_f32_16x16x32_bf16 %[o0], %[i0], %[i1], %[o0]\n\t" "ds_read_b64_tr_b16 %[o1], %[i2] offset:4096\n\t" "v_fma_f32 %[o2], %[o2], %[i3], %[i3]\n\t" : [o0] "+v"(e.acc[1][3]), [o1] "=&v"(e.whi[0]), [o2] "+v"(tz[0][3]) : [i0] "v"(wf), [i1] "a"(e.tok[2][0][1]), [i2] "v"(e.laddr[1][1]), [i3] "v"(sc.ri[0]));
  asm volatile("v_mfma_f32_16x16x32_bf16 %[o0], %[i0], %[i1], %[o0]\n\t" "v_fma_f32 %[o1], %[o1], %[i2], %[i2]\n\t" "v_rcp_f32 %[o2], %[o2]\n\t" "v_rcp_f32 %[o3], %[o3]\n\t" : [o0] "+v"(e.acc[2][3]), [o1] "+v"(tz[1][3]), [o2] "+v"(tz[0][0]), [o3] "+v"(tz[1][0]) : [i0] "v"(wf), [i1] "a"(e.tok[2][0][2]), [i2] "v"(sc.ri[1]));
  asm volatile("v_mfma_f32_16x16x32_bf16 %[o0], %[i0], %[i1], %[o0]\n\t" "v_rcp_f32 %[o1], %[o1]\n\t" "v_rcp_f32 %[o2], %[o2]\n\t" : [o0] "+v"(e.acc[3][3]), [o1] "+v"(tz[0][1]), [o2] "+v"(tz[1][1]) : [i0] "v"(wf), [i1] "a"(e.tok[2][0][3]));
  }
  asm volatile("s_waitcnt lgkmcnt(4)");
  { const u32x4 wf = __builtin_shufflevector(e.wlo[2], e.whi[2], 0, 1, 2, 3);
  asm volatile("v_mfma_f32_16x16x32_bf16 %[o0], %[i0], %[i1], %[o0]\n\t" "ds_read_b64_tr_b16 %[o1], %[i2] offset:8192\n\t" "v_rcp_f32 %[o2], %[o2]\n\t" : [o0] "+v"(e.acc[0][2]), [o1] "=&v"(e.wlo[1]), [o2] "+v"(tz[0][2]) : [i0] "v"(wf), [i1] "a"(e.tok[3][0][0]), [i2] "v"(e.laddr[1][1]));
  asm volatile("v_mfma_f32_16x16x32_bf16 %[o0], %[i0], %[i1], %[o0]\n\t" "ds_read_b64_tr_b16 %[o1], %[i2] offset:12288\n\t" "v_rcp_f32 %[o2], %[o2]\n\t" : [o0] "+v"(e.acc[1][2]), [o1] "=&v"(e.whi[1]), [o2] "+v"(tz[1][2]) : [i0] "v"(wf), [i1] "a"(e.tok[3][0][1]), [i2] "v"(e.laddr[1][1]));
  asm volatile("v_mfma_f32_16x16x32_bf16 %[o0], %[i0], %[i1], %[o0]\n\t" "v_rcp_f32 %[o1], %[o1]\n\t" "v_rcp_f32 %[o2], %[o2]\n\t" "v_mul_f32 %[o3], %[i2], %[i3]\n\t" : [o0] "+v"(e.acc[2][2]), [o1] "+v"(tz[0][3]), [o2] "+v"(tz[1][3]), [o3] "=&v"(tw[0][0]) : [i0] "v"(wf), [i1] "a"(e.tok[3][0][2]), [i2] "v"(e.acc[0][1][0]), [i3] "v"(tz[0][0]));
  asm volatile("v_mfma_f32_16x16x32_bf16 %[o0], %[i0], %[i1], %[o0]\n\t" "v_mul_f32 %[o1], %[i2], %[i3]\n\t" "v_mul_f32 %[o2], %[i4], %[i5]\n\t" : [o0] "+v"(e.acc[3][2]), [o1] "=&v"(tw[1][0]), [o2] "=&v"(tw[0][1]) : [i0] "v"(wf), [i1] "a"(e.tok[3][0][3]), [i2] "v"(e.acc[1][1][0]), [i3] "v"(tz[1][0]), [i4] "v"(e.acc[0][1][1]), [i5] "v"(tz[0][1]));
  }
  asm volatile("s_waitcnt lgkmcnt(4)");
  { const u32x4 wf = __builtin_shufflevector(e.wlo[3], e.whi[3], 0, 1, 2, 3);
  asm volatile("v_mfma_f32_16x16x32_bf16 %[o0], %[i0], %[i1], %[o0]\n\t" "ds_read_b64_tr_b16 %[o1], %[i2] offset:16384\n\t" "v_mul_f32 %[o2], %[i3], %[i4]\n\t" : [o0] "+v"(e.acc[0][3]), [o1] "=&v"(e.wlo[2]), [o2] "=&v"(tw[1][1]) : [i0] "v"(wf), [i1] "a"(e.tok[3][0][0]), [i2] "v"(e.laddr[1][1]), [i3] "v"(e.acc[1][1][1]), [i4] "v"(tz[1][1]));
  asm volatile("v_mfma_f32_16x16x32_bf16 %[o0], %[i0], %[i1], %[o0]\n\t" "ds_read_b64_tr_b16 %[o1], %[i2] offset:20480\n\t" "v_mul_f32 %[o2], %[i3], %[i4]\n\t" : [o0] "+v"(e.acc[1][3]), [o1] "=&v"(e.whi[2]), [o2] "=&v"(tw[0][2]) : [i0] "v"(wf), [i1] "a"(e.tok[3][0][1]), [i2] "v"(e.laddr[1][1]), [i3] "v"(e.acc[0][1][2]), [i4] "v"(tz[0][2]));
  asm volatile("v_mfma_f32_16x16x32_bf16 %[o0], %[i0], %[i1], %[o0]\n\t" "v_mul_f32 %[o1], %[i2], %[i3]\n\t" "v_mul_f32 %[o2], %[i4], %[i5]\n\t" "v_mul_f32 %[o3], %[i6], %[i7]\n\t" : [o0] "+v"(e.acc[2][3]), [o1] "=&v"(tw[1][2]), [o2] "=&v"(tw[0][3]), [o3] "=&v"(tw[1][3]) : [i0] "v"(wf), [i1] "a"(e.tok[3][0][2]), [i2] "v"(e.acc[1][1][2]), [i3] "v"(tz[1][2]), [i4] "v"(e.acc[0][1][3]), [i5] "v"(tz[0][3]), [i6] "v"(e.acc[1][1][3]), [i7] "v"(tz[1][3]));
  asm volatile("v_mfma_f32_16x16x32_bf16 %[o0], %[i0], %[i1], %[o0]\n\t" "v_cvt_pk_bf16_f32 %[o1], %[i2], %[i3]\n\t" "v_cvt_pk_bf16_f32 %[o2], %[i4], %[i5]\n\t" : [o0] "+v"(e.acc[3][3]), [o1] "=&v"(pk[0][0][0]), [o2] "=&v"(pk[0][1][0]) : [i0] "v"(wf), [i1] "a"(e.tok[3][0][3]), [i2] "v"(tw[0][0]), [i3] "v"(tw[0][1]), [i4] "v"(tw[1][0]), [i5] "v"(tw[1][1]));
  }
  asm volatile("s_waitcnt lgkmcnt(4)");
  { const u32x4 wf = __builtin_shufflevector(e.wlo[0], e.whi[0], 0, 1, 2, 3);
  asm volatile("v_mfma_f32_16x16x32_bf16 %[o0], %[i0], %[i1], %[o0]\n\t" "ds_read_b64_tr_b16 %[o1], %[i2] offset:24576\n\t" "v_cvt_pk_bf16_f32 %[o2], %[i3], %[i4]\n\t" : [o0] "+v"(e.acc[0][2]), [o1] "=&v"(e.wlo[3]), [o2] "=&v"(pk[0][0][1]) : [i0] "v"(wf), [i1] "a"(e.tok[4][0][0]), [i2] "v"(e.laddr[1][1]), [i3] "v"(tw[0][2]), [i4] "v"(tw[0][3]));
  asm volatile("v_mfma_f32_16x16x32_bf16 %[o0], %[i0], %[i1], %[o0]\n\t" "ds_read_b64_tr_b16 %[o1], %[i2] offset:28672\n\t" "v_cvt_pk_bf16_f32 %[o2], %[i3], %[i4]\n\t" : [o0] "+v"(e.acc[1][2]), [o1] "=&v"(e.whi[3]), [o2] "=&v"(pk[0][1][1]) : [i0] "v"(wf), [i1] "a"(e.tok[4][0][1]), [i2] "v"(e.laddr[1][1]), [i3] "v"(tw[1][2]), [i4] "v"(tw[1][3]));
  asm volatile("v_mfma_f32_16x16x32_bf16 %[o0], %[i0], %[i1], %[o0]\n\t" : [o0] "+v"(e.acc[2][2]) : [i0] "v"(wf), [i1] "a"(e.tok[4][0][2]));
  st64a(c.wsr, p0[0] + 0, so.first, (u32x2){pk[0][0][0], pk[0][0][1]});
  st64a(c.wsr, p0[0] + 1024, so.first, (u32x2){pk[0][1][0], pk[0][1][1]});
  asm volatile("v_mul_f32 %[o0], %[i0], %[i1]\n\t" : [o0] "=&v"(tz[0][0]) : [i0] "v"(e.acc[2][0][0]), [i1] "v"(sc.zr[2]));
  asm volatile("v_mfma_f32_16x16x32_bf16 %[o0], %[i0], %[i1], %[o0]\n\t" "v_mul_f32 %[o1], %[i2], %[i3]\n\t" "v_mul_f32 %[o2], %[i4], %[i5]\n\t" : [o0] "+v"(e.acc[3][2]), [o1] "=&v"(tz[1][0]), [o2] "=&v"(tz[0][1]) : [i0] "v"(wf), [i1] "a"(e.tok[4][0][3]), [i2] "v"(e.acc[3][0][0]), [i3] "v"(sc.zr[3]), [i4] "v"(e.acc[2][0][1]), [i5] "v"(sc.zr[2]));
  }
  asm volatile("s_waitcnt lgkmcnt(4)");
  { const u32x4 wf = __builtin_shufflevector(e.wlo[1], e.whi[1], 0, 1, 2, 3);
  asm volatile("v_mfma_f32_16x16x32_bf16 %[o0], %[i0], %[i1], %[o0]\n\t" "ds_read_b64_tr_b16 %[o1], %[i2] offset:32768\n\t" "v_mul_f32 %[o2], %[i3], %[i4]\n\t" : [o0] "+v"(e.acc[0][3]), [o1] "=&v"(e.wlo[0]), [o2] "=&v"(tz[1][1]) : [i0] "v"(wf), [i1] "a"(e.tok[4][0][0]), [i2] "v"(e.laddr[1][1]), [i3] "v"(e.acc[3][0][1]), [i4] "v"(sc.zr[3]));
  asm volatile("v_mfma_f32_16x16x32_bf16 %[o0], %[i0], %[i1], %[o0]\n\t" "ds_read_b64_tr_b16 %[o1], %[i2] offset:36864\n\t" "v_mul_f32 %[o2], %[i3], %[i4]\n\t" : [o0] "+v"(e.acc[1][3]), [o1] "=&v"(e.whi[0]), [o2] "=&v"(tz[0][2]) : [i0] "v"(wf), [i1] "a"(e.tok[4][0][1]), [i2] "v"(e.laddr[1][1]), [i3] "v"(e.acc[2][0][2]), [i4] "v"(sc.zr[2]));
  asm volatile("v_mfma_f32_16x16x32_bf16 %[o0], %[i0], %[i1], %[o0]\n\t" "v_mul_f32 %[o1], %[i2], %[i3]\n\t" "v_mul_f32 %[o2], %[i4], %[i5]\n\t" "v_mul_f32 %[o3], %[i6], %[i7]\n\t" : [o0] "+v"(e.acc[2][3]), [o1] "=&v"(tz[1][2]), [o2] "=&v"(tz[0][3]), [o3] "=&v"(tz[1][3]) : [i0] "v"(wf), [i1] "a"(e.tok[4][0][2]), [i2] "v"(e.acc[3][0][2]), [i3] "v"(sc.zr[3]), [i4] "v"(e.acc[2][0][3]), [i5] "v"(sc.zr[2]), [i6] "v"(e.acc[3][0][3]), [i7] "v"(sc.zr[3]));
  asm volatile("v_mfma_f32_16x16x32_bf16 %[o0], %[i0], %[i1], %[o0]\n\t" "v_exp_f32 %[o1], %[o1]\n\t" "v_exp_f32 %[o2], %[o2]\n\t" : [o0] "+v"(e.acc[3][3]), [o1] "+v"(tz[0][0]), [o2] "+v"(tz[1][0]) : [i0] "v"(wf), [i1] "a"(e.tok[4][0][3]));
  }
  asm volatile("s_waitcnt lgkmcnt(4)");
  { const u32x4 wf = __builtin_shufflevector(e.wlo[2], e.whi[2], 0, 1, 2, 3);
  asm volatile("v_mfma_f32_16x16x32_bf16 %[o0], %[i0], %[i1], %[o0]\n\t" "ds_read_b64_tr_b16 %[o1], %[i2] offset:40960\n\t" "v_exp_f32 %[o2], %[o2]\n\t" : [o0] "+v"(e.acc[0][2]), [o1] "=&v"(e.wlo[1]), [o2] "+v"(tz[0][1]) : [i0] "v"(wf), [i1] "a"(e.tok[5][0][0]), [i2] "v"(e.laddr[1][1]));
  asm volatile("v_mfma_f32_16x16x32_bf16 %[o0], %[i0], %[i1], %[o0]\n\t" "ds_read_b64_tr_b16 %[o1], %[i2] offset:45056\n\t" "v_exp_f32 %[o2], %[o2]\n\t" : [o0] "+v"(e.acc[1][2]), [o1] "=&v"(e.whi[1]), [o2] "+v"(tz[1][1]) : [i0] "v"(wf), [i1] "a"(e.tok[5][0][1]), [i2] "v"(e.laddr[1][1]));
  asm volatile("v_mfma_f32_16x16x32_bf16 %[o0], %[i0], %[i1], %[o0]\n\t" "v_exp_f32 %[o1], %[o1]\n\t" "v_exp_f32 %[o2], %[o2]\n\t" "v_exp_f32 %[o3], %[o3]\n\t" : [o0] "+v"(e.acc[2][2]), [o1] "+v"(tz[0][2]), [o2] "+v"(tz[1][2]), [o3] "+v"(tz[0][3]) : [i0] "v"(wf), [i1] "a"(e.tok[5][0][2]));
  asm volatile("v_mfma_f32_16x16x32_bf16 %[o0], %[i0], %[i1], %[o0]\n\t" "v_exp_f32 %[o1], %[o1]\n\t" "v_fma_f32 %[o2], %[o2], %[i2], %[i2]\n\t" : [o0] "+v"(e.acc[3][2]), [o1] "+v"(tz[1][3]), [o2] "+v"(tz[0][0]) : [i0] "v"(wf), [i1] "a"(e.tok[5][0][3]), [i2] "v"(sc.ri[2]));
  }
  asm volatile("s_waitcnt lgkmcnt(4)");
  { const u32x4 wf = __builtin_shufflevector(e.wlo[3], e.whi[3], 0, 1, 2, 3);
  asm volatile("v_mfma_f32_16x16x32_bf16 %[o0], %[i0], %[i1], %[o0]\n\t" "ds_read_b64_tr_b16 %[o1], %[i2] offset:49152\n\t" "v_fma_f32 %[o2], %[o2], %[i3], %[i3]\n\t" : [o0] "+v"(e.acc[0][3]), [o1] "=&v"(e.wlo[2]), [o2] "+v"(tz[1][0]) : [i0] "v"(wf), [i1] "a"(e.tok[5][0][0]), [i2] "v"(e.laddr[1][1]), [i3] "v"(sc.ri[3]));
  asm volatile("v_mfma_f32_16x16x32_bf16 %[o0], %[i0], %[i1], %[o0]\n\t" "ds_read_b64_tr_b16 %[o1], %[i2] offset:53248\n\t" "v_fma_f32 %[o2], %[o2], %[i3], %[i3]\n\t" : [o0] "+v"(e.acc[1][3]), [o1] "=&v"(e.whi[2]), [o2] "+v"(tz[0][1]) : [i0] "v"(wf), [i1] "a"(e.tok[5][0][1]), [i2] "v"(e.laddr[1][1]), [i3] "v"(sc.ri[2]));
  asm volatile("v_mfma_f32_16x16x32_bf16 %[o0], %[i0], %[i1], %[o0]\n\t" "v_fma_f32 %[o1], %[o1], %[i2], %[i2]\n\t" "v_fma_f32 %[o2], %[o2], %[i3], %[i3]\n\t" "v_fma_f32 %[o3], %[o3], %[i4], %[i4]\n\t" : [o0] "+v"(e.acc[2][3]), [o1] "+v"(tz[1][1]), [o2] "+v"(tz[0][2]), [o3] "+v"(tz[1][2]) : [i0] "v"(wf), [i1] "a"(e.tok[5][0][2]), [i2] "v"(sc.ri[3]), [i3] "v"(sc.ri[2]), [i4] "v"(sc.ri[3]));
  asm volatile("v_mfma_f32_16x16x32_bf16 %[o0], %[i0], %[i1], %[o0]\n\t" "v_fma_f32 %[o1], %[o1], %[i2], %[i2]\n\t" "v_fma_f32 %[o2], %[o2], %[i3], %[i3]\n\t" : [o0] "+v"(e.acc[3][3]), [o1] "+v"(tz[0][3]), [o2] "+v"(tz[1][3]) : [i0] "v"(wf), [i1] "a"(e.tok[5][0][3]), [i2] "v"(sc.ri[2]), [i3] "v"(sc.ri[3]));
  }
  asm volatile("s_waitcnt lgkmcnt(4)");
  { const u32x4 wf = __builtin_shufflevector(e.wlo[0], e.whi[0], 0, 1, 2, 3);
  asm volatile("v_mfma_f32_16x16x32_bf16 %[o0], %[i0], %[i1], %[o0]\n\t" "ds_read_b64_tr_b16 %[o1], %[i2] offset:57344\n\t" "v_rcp_f32 %[o2], %[o2]\n\t" : [o0] "+v"(e.acc[0][2]), [o1] "=&v"(e.wlo[3]), [o2] "+v"(tz[0][0]) : [i0] "v"(wf), [i1] "a"(e.tok[6][0][0]), [i2] "v"(e.laddr[1][1]));
  asm volatile("v_mfma_f32_16x16x32_bf16 %[o0], %[i0], %[i1], %[o0]\n\t" "ds_read_b64_tr_b16 %[o1], %[i2] offset:61440\n\t" "v_rcp_f32 %[o2], %[o2]\n\t" : [o0] "+v"(e.acc[1][2]), [o1] "=&v"(e.whi[3]), [o2] "+v"(tz[1][0]) : [i0] "v"(wf), [i1] "a"(e.tok[6][0][1]), [i2] "v"(e.laddr[1][1]));
  asm volatile("v_mfma_f32_16x16x32_bf16 %[o0], %[i0], %[i1], %[o0]\n\t" "v_rcp_f32 %[o1], %[o1]\n\t" "v_rcp_f32 %[o2], %[o2]\n\t" "v_rcp_f32 %[o3], %[o3]\n\t" : [o0] "+v"(e.acc[2][2]), [o1] "+v"(tz[0][1]), [o2] "+v"(tz[1][1]), [o3] "+v"(tz[0][2]) : [i0] "v"(wf), [i1] "a"(e.tok[6][0][2]));
  asm volatile("v_mfma_f32_16x16x32_bf16 %[o0], %[i0], %[i1], %[o0]\n\t" "v_rcp_f32 %[o1], %[o1]\n\t" "v_rcp_f32 %[o2], %[o2]\n\t" : [o0] "+v"(e.acc[3][2]), [o1] "+v"(tz[1][2]), [o2] "+v"(tz[0][3]) : [i0] "v"(wf), [i1] "a"(e.tok[6][0][3]));
  }
  asm volatile("s_waitcnt lgkmcnt(4)");
  { const u32x4 wf = __builtin_shufflevector(e.wlo[1], e.whi[1], 0, 1, 2, 3);
  asm volatile("v_mfma_f32_16x16x32_bf16 %[o0], %[i0], %[i1], %[o0]\n\t" "ds_read_b64_tr_b16 %[o1], %[i2] offset:16384\n\t" "v_rcp_f32 %[o2], %[o2]\n\t" : [o0] "+v"(e.acc[0][3]), [o1] "=&v"(e.wlo[0]), [o2] "+v"(tz[1][3]) : [i0] "v"(wf), [i1] "a"(e.tok[6][0][0]), [i2] "v"(e.laddr[0][2]));
  asm volatile("v_mfma_f32_16x16x32_bf16 %[o0], %[i0], %[i1], %[o0]\n\t" "ds_read_b64_tr_b16 %[o1], %[i2] offset:20480\n\t" "v_mul_f32 %[o2], %[i3], %[i4]\n\t" : [o0] "+v"(e.acc[1][3]), [o1] "=&v"(e.whi[0]), [o2] "=&v"(tw[0][0]) : [i0] "v"(wf), [i1] "a"(e.tok[6][0][1]), [i2] "v"(e.laddr[0][2]), [i3] "v"(e.acc[2][1][0]), [i4] "v"(tz[0][0]));
  asm volatile("v_mfma_f32_16x16x32_bf16 %[o0], %[i0], %[i1], %[o0]\n\t" "v_mul_f32 %[o1], %[i2], %[i3]\n\t" "v_mul_f32 %[o2], %[i4], %[i5]\n\t" "v_mul_f32 %[o3], %[i6], %[i7]\n\t" : [o0] "+v"(e.acc[2][3]), [o1] "=&v"(tw[1][0]), [o2] "=&v"(tw[0][1]), [o3] "=&v"(tw[1][1]) : [i0] "v"(wf), [i1] "a"(e.tok[6][0][2]), [i2] "v"(e.acc[3][1][0]), [i3] "v"(tz[1][0]), [i4] "v"(e.acc[2][1][1]), [i5] "v"(tz[0][1]), [i6] "v"(e.acc[3][1][1]), [i7] "v"(tz[1][1]));
  asm volatile("v_mfma_f32_16x16x32_bf16 %[o0], %[i0], %[i1], %[o0]\n\t" "v_mul_f32 %[o1], %[i2], %[i3]\n\t" "v_mul_f32 %[o2], %[i4], %[i5]\n\t" : [o0] "+v"(e.acc[3][3]), [o1] "=&v"(tw[0][2]), [o2] "=&v"(tw[1][2]) : [i0] "v"(wf), [i1] "a"(e.tok[6][0][3]), [i2] "v"(e.acc[2][1][2]), [i3] "v"(tz[0][2]), [i4] "v"(e.acc[3][1][2]), [i5] "v"(tz[1][2]));
  }
  asm volatile("s_waitcnt lgkmcnt(4)");
  { const u32x4 wf = __builtin_shufflevector(e.wlo[2], e.whi[2], 0, 1, 2, 3);
  asm volatile("v_mfma_f32_16x16x32_bf16 %[o0], %[i0], %[i1], %[o0]\n\t" "ds_read_b64_tr_b16 %[o1], %[i2] offset:24576\n\t" "v_mul_f32 %[o2], %[i3], %[i4]\n\t" : [o0] "+v"(e.acc[0][2]), [o1] "=&v"(e.wlo[1]), [o2] "=&v"(tw[0][3]) : [i0] "v"(wf), [i1] "a"(e.tok[7][0][0]), [i2] "v"(e.laddr[0][2]), [i3] "v"(e.acc[2][1][3]), [i4] "v"(tz[0][3]));
  asm volatile("v_mfma_f32_16x16x32_bf16 %[o0], %[i0], %[i1], %[o0]\n\t" "ds_read_b64_tr_b16 %[o1], %[i2] offset:28672\n\t" "v_mul_f32 %[o2], %[i3], %[i4]\n\t" : [o0] "+v"(e.acc[1][2]), [o1] "=&v"(e.whi[1]), [o2] "=&v"(tw[1][3]) : [i0] "v"(wf), [i1] "a"(e.tok[7][0][1]), [i2] "v"(e.laddr[0][2]), [i3] "v"(e.acc[3][1][3]), [i4] "v"(tz[1][3]));
  asm volatile("v_mfma_f32_16x16x32_bf16 %[o0], %[i0], %[i1], %[o0]\n\t" "v_cvt_pk_bf16_f32 %[o1], %[i2], %[i3]\n\t" "v_cvt_pk_bf16_f32 %[o2], %[i4], %[i5]\n\t" "v_cvt_pk_bf16_f32 %[o3], %[i6], %[i7]\n\t" : [o0] "+v"(e.acc[2][2]), [o1] "=&v"(pk[0][2][0]), [o2] "=&v"(pk[0][3][0]), [o3] "=&v"(pk[0][2][1]) : [i0] "v"(wf), [i1] "a"(e.tok[7][0][2]), [i2] "v"(tw[0][0]), [i3] "v"(tw[0][1]), [i4] "v"(tw[1][0]), [i5] "v"(tw[1][1]), [i6] "v"(tw[0][2]), [i7] "v"(tw[0][3]));
  asm volatile("v_mfma_f32_16x16x32_bf16 %[o0], %[i0], %[i1], %[o0]\n\t" "v_cvt_pk_bf16_f32 %[o1], %[i2], %[i3]\n\t" : [o0] "+v"(e.acc[3][2]), [o1] "=&v"(pk[0][3][1]) : [i0] "v"(wf), [i1] "a"(e.tok[7][0][3]), [i2] "v"(tw[1][2]), [i3] "v"(tw[1][3]));
  st64a(c.wsr, p0[1] + 0, so.first, (u32x2){pk[0][2][0], pk[0][2][1]});
  }
  asm volatile("s_waitcnt lgkmcnt(4)");
  { const u32x4 wf = __builtin_shufflevector(e.wlo[3], e.whi[3], 0, 1, 2, 3);
  asm volatile("v_mfma_f32_16x16x32_bf16 %[o0], %[i0], %[i1], %[o0]\n\t" "ds_read_b64_tr_b16 %[o1], %[i2] offset:32768\n\t" : [o0] "+v"(e.acc[0][3]), [o1] "=&v"(e.wlo[2]) : [i0] "v"(wf), [i1] "a"(e.tok[7][0][0]), [i2] "v"(e.laddr[0][2]));
  st64a(c.wsr, p0[1] + 1024, so.first, (u32x2){pk[0][3][0], pk[0][3][1]});
  asm volatile("v_mfma_f32_16x16x32_bf16 %[o0], %[i0], %[i1], %[o0]\n\t" "ds_read_b64_tr_b16 %[o1], %[i2] offset:36864\n\t" : [o0] "+v"(e.acc[1][3]), [o1] "=&v"(e.whi[2]) : [i0] "v"(wf), [i1] "a"(e.tok[7][0][1]), [i2] "v"(e.laddr[0][2]));
  asm volatile("v_mfma_f32_16x16x32_bf16 %[o0], %[i0], %[i1], %[o0]\n\t" : [o0] "+v"(e.acc[2][3]) : [i0] "v"(wf), [i1] "a"(e.tok[7][0][2]));
  asm volatile("v_mfma_f32_16x16x32_bf16 %[o0], %[i0], %[i1], %[o0]\n\t" : [o0] "+v"(e.acc[3][3]) : [i0] "v"(wf), [i1] "a"(e.tok[7][0][3]));
  }
  asm volatile("s_waitcnt lgkmcnt(4)");
  { const u32x4 wf = __builtin_shufflevector(e.wlo[0], e.whi[0], 0, 1, 2, 3);
  asm volatile("v_mfma_f32_16x16x32_bf16 %[o0], %[i0], %[i1], %[o0]\n\t" "ds_read_b64_tr_b16 %[o1], %[i2] offset:40960\n\t" "v_mul_f32 %[o2], %[i3], %[i4]\n\t" : [o0] "+v"(e.acc[0][4]), [o1] "=&v"(e.wlo[3]), [o2] "=&v"(tz[0][0]) : [i0] "v"(wf), [i1] "a"(e.tok[1][0][0]), [i2] "v"(e.laddr[0][2]), [i3] "v"(e.acc[0][2][0]), [i4] "v"(sc.zr[0]));
  asm volatile("v_mfma_f32_16x16x32_bf16 %[o0], %[i0], %[i1], %[o0]\n\t" "ds_read_b64_tr_b16 %[o1], %[i2] offset:45056\n\t" "v_mul_f32 %[o2], %[i3], %[i4]\n\t" : [o0] "+v"(e.acc[1][4]), [o1] "=&v"(e.whi[3]), [o2] "=&v"(tz[1][0]) : [i0] "v"(wf), [i1] "a"(e.tok[1][0][1]), [i2] "v"(e.laddr[0][2]), [i3] "v"(e.acc[1][2][0]), [i4] "v"(sc.zr[1]));
  asm volatile("v_mfma_f32_16x16x32_bf16 %[o0], %[i0], %[i1], %[o0]\n\t" "v_mul_f32 %[o1], %[i2], %[i3]\n\t" "v_mul_f32 %[o2], %[i4], %[i5]\n\t" "v_mul_f32 %[o3], %[i6], %[i7]\n\t" : [o0] "+v"(e.acc[2][4]), [o1] "=&v"(tz[0][1]), [o2] "=&v"(tz[1][1]), [o3] "=&v"(tz[0][2]) : [i0] "v"(wf), [i1] "a"(e.tok[1][0][2]), [i2] "v"(e.acc[0][2][1]), [i3] "v"(sc.zr[0]), [i4] "v"(e.acc[1][2][1]), [i5] "v"(sc.zr[1]), [i6] "v"(e.acc[0][2][2]), [i7] "v"(sc.zr[0]));
  asm volatile("v_mfma_f32_16x16x32_bf16 %[o0], %[i0], %[i1], %[o0]\n\t" "v_mul_f32 %[o1], %[i2], %[i3]\n\t" "v_mul_f32 %[o2], %[i4], %[i5]\n\t" : [o0] "+v"(e.acc[3][4]), [o1] "=&v"(tz[1][2]), [o2] "=&v"(tz[0][3]) : [i0] "v"(wf), [i1] "a"(e.tok[1][0][3]), [i2] "v"(e.acc[1][2][2]), [i3] "v"(sc.zr[1]), [i4] "v"(e.acc[0][2][3]), [i5] "v"(sc.zr[0]));
  }
  asm volatile("s_waitcnt lgkmcnt(4)");
  { const u32x4 wf = __builtin_shufflevector(e.wlo[1], e.whi[1], 0, 1, 2, 3);
  asm volatile("v_mfma_f32_16x16x32_bf16 %[o0], %[i0], %[i1], %[o0]\n\t" "ds_read_b64_tr_b16 %[o1], %[i2] offset:49152\n\t" "v_mul_f32 %[o2], %[i3], %[i4]\n\t" : [o0] "+v"(e.acc[0][5]), [o1] "=&v"(e.wlo[0]), [o2] "=&v"(tz[1][3]) : [i0] "v"(wf), [i1] "a"(e.tok[1][0][0]), [i2] "v"(e.laddr[0][2]), [i3] "v"(e.acc[1][2][3]), [i4] "v"(sc.zr[1]));
  asm volatile("v_mfma_f32_16x16x32_bf16 %[o0], %[i0], %[i1], %[o0]\n\t" "ds_read_b64_tr_b16 %[o1], %[i2] offset:53248\n\t" "v_exp_f32 %[o2], %[o2]\n\t" : [o0] "+v"(e.acc[1][5]), [o1] "=&v"(e.whi[0]), [o2] "+v"(tz[0][0]) : [i0] "v"(wf), [i1] "a"(e.tok[1][0][1]), [i2] "v"(e.laddr[0][2]));
  asm volatile("v_mfma_f32_16x16x32_bf16 %[o0], %[i0], %[i1], %[o0]\n\t" "v_exp_f32 %[o1], %[o1]\n\t" "v_exp_f32 %[o2], %[o2]\n\t" "v_exp_f32 %[o3], %[o3]\n\t" : [o0] "+v"(e.acc[2][5]), [o1] "+v"(tz[1][0]), [o2] "+v"(tz[0][1]), [o3] "+v"(tz[1][1]) : [i0] "v"(wf), [i1] "a"(e.tok[1][0][2]));
  asm volatile("v_mfma_f32_16x16x32_bf16 %[o0], %[i0], %[i1], %[o0]\n\t" "v_exp_f32 %[o1], %[o1]\n\t" "v_exp_f32 %[o2], %[o2]\n\t" : [o0] "+v"(e.acc[3][5]), [o1] "+v"(tz[0][2]), [o2] "+v"(tz[1][2]) : [i0] "v"(wf), [i1] "a"(e.tok[1][0][3]));
  }
  asm volatile("s_waitcnt lgkmcnt(4)");
  { const u32x4 wf = __builtin_shufflevector(e.wlo[2], e.whi[2], 0, 1, 2, 3);
  asm volatile("v_mfma_f32_16x16x32_bf16 %[o0], %[i0], %[i1], %[o0]\n\t" "ds_read_b64_tr_b16 %[o1], %[i2] offset:57344\n\t" "v_exp_f32 %[o2], %[o2]\n\t" : [o0] "+v"(e.acc[0][4]), [o1] "=&v"(e.wlo[1]), [o2] "+v"(tz[0][3]) : [i0] "v"(wf), [i1] "a"(e.tok[2][0][0]), [i2] "v"(e.laddr[0][2]));
  asm volatile("v_mfma_f32_16x16x32_bf16 %[o0], %[i0], %[i1], %[o0]\n\t" "ds_read_b64_tr_b16 %[o1], %[i2] offset:61440\n\t" "v_exp_f32 %[o2], %[o2]\n\t" : [o0] "+v"(e.acc[1][4]), [o1] "=&v"(e.whi[1]), [o2] "+v"(tz[1][3]) : [i0] "v"(wf), [i1] "a"(e.tok[2][0][1]), [i2] "v"(e.laddr[0][2]));
  asm volatile("v_mfma_f32_16x16x32_bf16 %[o0], %[i0], %[i1], %[o0]\n\t" "v_fma_f32 %[o1], %[o1], %[i2], %[i2]\n\t" "v_fma_f32 %[o2], %[o2], %[i3], %[i3]\n\t" "v_fma_f32 %[o3], %[o3], %[i4], %[i4]\n\t" : [o0] "+v"(e.acc[2][4]), [o1] "+v"(tz[0][0]), [o2] "+v"(tz[1][0]), [o3] "+v"(tz[0][1]) : [i0] "v"(wf), [i1] "a"(e.tok[2][0][2]), [i2] "v"(sc.ri[0]), [i3] "v"(sc.ri[1]), [i4] "v"(sc.ri[0]));
  asm volatile("v_mfma_f32_16x16x32_bf16 %[o0], %[i0], %[i1], %[o0]\n\t" "v_fma_f32 %[o1], %[o1], %[i2], %[i2]\n\t" "v_fma_f32 %[o2], %[o2], %[i3], %[i3]\n\t" : [o0] "+v"(e.acc[3][4]), [o1] "+v"(tz[1][1]), [o2] "+v"(tz[0][2]) : [i0] "v"(wf), [i1] "a"(e.tok[2][0][3]), [i2] "v"(sc.ri[1]), [i3] "v"(sc.ri[0]));
  }
  asm volatile("s_waitcnt lgkmcnt(4)");
  { const u32x4 wf = __builtin_shufflevector(e.wlo[3], e.whi[3], 0, 1, 2, 3);
  asm volatile("v_mfma_f32_16x16x32_bf16 %[o0], %[i0], %[i1], %[o0]\n\t" "ds_read_b64_tr_b16 %[o1], %[i2] offset:0\n\t" "v_fma_f32 %[o2], %[o2], %[i3], %[i3]\n\t" : [o0] "+v"(e.acc[0][5]), [o1] "=&v"(e.wlo[2]), [o2] "+v"(tz[1][2]) : [i0] "v"(wf), [i1] "a"(e.tok[2][0][0]), [i2] "v"(e.laddr[1][2]), [i3] "v"(sc.ri[1]));
  asm volatile("v_mfma_f32_16x16x32_bf16 %[o0], %[i0], %[i1], %[o0]\n\t" "ds_read_b64_tr_b16 %[o1], %[i2] offset:4096\n\t" "v_fma_f32 %[o2], %[o2], %[i3], %[i3]\n\t" : [o0] "+v"(e.acc[1][5]), [o1] "=&v"(e.whi[2]), [o2] "+v"(tz[0][3]) : [i0] "v"(wf), [i1] "a"(e.tok[2][0][1]), [i2] "v"(e.laddr[1][2]), [i3] "v"(sc.ri[0]));
  asm volatile("v_mfma_f32_16x16x32_bf16 %[o0], %[i0], %[i1], %[o0]\n\t" "v_fma_f32 %[o1], %[o1], %[i2], %[i2]\n\t" "v_rcp_f32 %[o2], %[o2]\n\t" "v_rcp_f32 %[o3], %[o3]\n\t" : [o0] "+v"(e.acc[2][5]), [o1] "+v"(tz[1][3]), [o2] "+v"(tz[0][0]), [o3] "+v"(tz[1][0]) : [i0] "v"(wf), [i1] "a"(e.tok[2][0][2]), [i2] "v"(sc.ri[1]));
  asm volatile("v_mfma_f32_16x16x32_bf16 %[o0], %[i0], %[i1], %[o0]\n\t" "v_rcp_f32 %[o1], %[o1]\n\t" "v_rcp_f32 %[o2], %[o2]\n\t" : [o0] "+v"(e.acc[3][5]), [o1] "+v"(tz[0][1]), [o2] "+v"(tz[1][1]) : [i0] "v"(wf), [i1] "a"(e.tok[2][0][3]));
  }
  asm volatile("s_waitcnt lgkmcnt(4)");
  { const u32x4 wf = __builtin_shufflevector(e.wlo[0], e.whi[0], 0, 1, 2, 3);
  asm volatile("v_mfma_f32_16x16x32_bf16 %[o0], %[i0], %[i1], %[o0]\n\t" "ds_read_b64_tr_b16 %[o1], %[i2] offset:8192\n\t" "v_rcp_f32 %[o2], %[o2]\n\t" : [o0] "+v"(e.acc[0][4]), [o1] "=&v"(e.wlo[3]), [o2] "+v"(tz[0][2]) : [i0] "v"(wf), [i1] "a"(e.tok[3][0][0]), [i2] "v"(e.laddr[1][2]));
  asm volatile("v_mfma_f32_16x16x32_bf16 %[o0], %[i0], %[i1], %[o0]\n\t" "ds_read_b64_tr_b16 %[o1], %[i2] offset:12288\n\t" "v_rcp_f32 %[o2], %[o2]\n\t" : [o0] "+v"(e.acc[1][4]), [o1] "=&v"(e.whi[3]), [o2] "+v"(tz[1][2]) : [i0] "v"(wf), [i1] "a"(e.tok[3][0][1]), [i2] "v"(e.laddr[1][2]));
  asm volatile("v_mfma_f32_16x16x32_bf16 %[o0], %[i0], %[i1], %[o0]\n\t" "v_rcp_f32 %[o1], %[o1]\n\t" "v_rcp_f32 %[o2], %[o2]\n\t" "v_mul_f32 %[o3], %[i2], %[i3]\n\t" : [o0] "+v"(e.acc[2][4]), [o1] "+v"(tz[0][3]), [o2] "+v"(tz[1][3]), [o3] "=&v"(tw[0][0]) : [i0] "v"(wf), [i1] "a"(e.tok[3][0][2]), [i2] "v"(e.acc[0][3][0]), [i3] "v"(tz[0][0]));
  asm volatile("v_mfma_f32_16x16x32_bf16 %[o0], %[i0], %[i1], %[o0]\n\t" "v_mul_f32 %[o1], %[i2], %[i3]\n\t" "v_mul_f32 %[o2], %[i4], %[i5]\n\t" : [o0] "+v"(e.acc[3][4]), [o1] "=&v"(tw[1][0]), [o2] "=&v"(tw[0][1]) : [i0] "v"(wf), [i1] "a"(e.tok[3][0][3]), [i2] "v"(e.acc[1][3][0]), [i3] "v"(tz[1][0]), [i4] "v"(e.acc[0][3][1]), [i5] "v"(tz[0][1]));
  }
  asm volatile("s_waitcnt lgkmcnt(4)");
  { const u32x4 wf = __builtin_shufflevector(e.wlo[1], e.whi[1], 0, 1, 2, 3);
  asm volatile("v_mfma_f32_16x16x32_bf16 %[o0], %[i0], %[i1], %[o0]\n\t" "ds_read_b64_tr_b16 %[o1], %[i2] offset:16384\n\t" "v_mul_f32 %[o2], %[i3], %[i4]\n\t" : [o0] "+v"(e.acc[0][5]), [o1] "=&v"(e.wlo[0]), [o2] "=&v"(tw[1][1]) : [i0] "v"(wf), [i1] "a"(e.tok[3][0][0]), [i2] "v"(e.laddr[1][2]), [i3] "v"(e.acc[1][3][1]), [i4] "v"(tz[1][1]));
  asm volatile("v_mfma_f32_16x16x32_bf16 %[o0], %[i0], %[i1], %[o0]\n\t" "ds_read_b64_tr_b16 %[o1], %[i2] offset:20480\n\t" "v_mul_f32 %[o2], %[i3], %[i4]\n\t" : [o0] "+v"(e.acc[1][5]), [o1] "=&v"(e.whi[0]), [o2] "=&v"(tw[0][2]) : [i0] "v"(wf), [i1] "a"(e.tok[3][0][1]), [i2] "v"(e.laddr[1][2]), [i3] "v"(e.acc[0][3][2]), [i4] "v"(tz[0][2]));
  asm volatile("v_mfma_f32_16x16x32_bf16 %[o0], %[i0], %[i1], %[o0]\n\t" "v_mul_f32 %[o1], %[i2], %[i3]\n\t" "v_mul_f32 %[o2], %[i4], %[i5]\n\t" "v_mul_f32 %[o3], %[i6], %[i7]\n\t" : [o0] "+v"(e.acc[2][5]), [o1] "=&v"(tw[1][2]), [o2] "=&v"(tw[0][3]), [o3] "=&v"(tw[1][3]) : [i0] "v"(wf), [i1] "a"(e.tok[3][0][2]), [i2] "v"(e.acc[1][3][2]), [i3] "v"(tz[1][2]), [i4] "v"(e.acc[0][3][3]), [i5] "v"(tz[0][3]), [i6] "v"(e.acc[1][3][3]), [i7] "v"(tz[1][3]));
  asm volatile("v_mfma_f32_16x16x32_bf16 %[o0], %[i0], %[i1], %[o0]\n\t" "v_cvt_pk_bf16_f32 %[o1], %[i2], %[i3]\n\t" "v_cvt_pk_bf16_f32 %[o2], %[i4], %[i5]\n\t" : [o0] "+v"(e.acc[3][5]), [o1] "=&v"(pk[1][0][0]), [o2] "=&v"(pk[1][1][0]) : [i0] "v"(wf), [i1] "a"(e.tok[3][0][3]), [i2] "v"(tw[0][0]), [i3] "v"(tw[0][1]), [i4] "v"(tw[1][0]), [i5] "v"(tw[1][1]));
  }
  asm volatile("s_waitcnt lgkmcnt(4)");
  { const u32x4 wf = __builtin_shufflevector(e.wlo[2], e.whi[2], 0, 1, 2, 3);
  asm volatile("v_mfma_f32_16x16x32_bf16 %[o0], %[i0], %[i1], %[o0]\n\t" "ds_read_b64_tr_b16 %[o1], %[i2] offset:24576\n\t" "v_cvt_pk_bf16_f32 %[o2], %[i3], %[i4]\n\t" : [o0] "+v"(e.acc[0][4]), [o1] "=&v"(e.wlo[1]), [o2] "=&v"(pk[1][0][1]) : [i0] "v"(wf), [i1] "a"(e.tok[4][0][0]), [i2] "v"(e.laddr[1][2]), [i3] "v"(tw[0][2]), [i4] "v"(tw[0][3]));
  asm volatile("v_mfma_f32_16x16x32_bf16 %[o0], %[i0], %[i1], %[o0]\n\t" "ds_read_b64_tr_b16 %[o1], %[i2] offset:28672\n\t" "v_cvt_pk_bf16_f32 %[o2], %[i3], %[i4]\n\t" : [o0] "+v"(e.acc[1][4]), [o1] "=&v"(e.whi[1]), [o2] "=&v"(pk[1][1][1]) : [i0] "v"(wf), [i1] "a"(e.tok[4][0][1]), [i2] "v"(e.laddr[1][2]), [i3] "v"(tw[1][2]), [i4] "v"(tw[1][3]));
  asm volatile("v_mfma_f32_16x16x32_bf16 %[o0], %[i0], %[i1], %[o0]\n\t" : [o0] "+v"(e.acc[2][4]) : [i0] "v"(wf), [i1] "a"(e.tok[4][0][2]));
  st128a(c.wsr, p0[0] + 0, so.pair[1], (u32x4){pk[0][0][0], pk[0][0][1], pk[1][0][0], pk[1][0][1]});
  st128a(c.wsr, p0[0] + 1024, so.pair[1], (u32x4){pk[0][1][0], pk[0][1][1], pk[1][1][0], pk[1][1][1]});
  asm volatile("v_mul_f32 %[o0], %[i0], %[i1]\n\t" : [o0] "=&v"(tz[0][0]) : [i0] "v"(e.acc[2][2][0]), [i1] "v"(sc.zr[2]));
  asm volatile("v_mfma_f32_16x16x32_bf16 %[o0], %[i0], %[i1], %[o0]\n\t" "v_mul_f32 %[o1], %[i2], %[i3]\n\t" "v_mul_f32 %[o2], %[i4], %[i5]\n\t" : [o0] "+v"(e.acc[3][4]), [o1] "=&v"(tz[1][0]), [o2] "=&v"(tz[0][1]) : [i0] "v"(wf), [i1] "a"(e.tok[4][0][3]), [i2] "v"(e.acc[3][2][0]), [i3] "v"(sc.zr[3]), [i4] "v"(e.acc[2][2][1]), [i5] "v"(sc.zr[2]));
  }
  asm volatile("s_waitcnt lgkmcnt(4)");
  { const u32x4 wf = __builtin_shufflevector(e.wlo[3], e.whi[3], 0, 1, 2, 3);
  asm volatile("v_mfma_f32_16x16x32_bf16 %[o0], %[i0], %[i1], %[o0]\n\t" "ds_read_b64_tr_b16 %[o1], %[i2] offset:32768\n\t" "v_mul_f32 %[o2], %[i3], %[i4]\n\t" : [o0] "+v"(e.acc[0][5]), [o1] "=&v"(e.wlo[2]), [o2] "=&v"(tz[1][1]) : [i0] "v"(wf), [i1] "a"(e.tok[4][0][0]), [i2] "v"(e.laddr[1][2]), [i3] "v"(e.acc[3][2][1]), [i4] "v"(sc.zr[3]));
  asm volatile("v_mfma_f32_16x16x32_bf16 %[o0], %[i0], %[i1], %[o0]\n\t" "ds_read_b64_tr_b16 %[o1], %[i2] offset:36864\n\t" "v_mul_f32 %[o2], %[i3], %[i4]\n\t" : [o0] "+v"(e.acc[1][5]), [o1] "=&v"(e.whi[2]), [o2] "=&v"(tz[0][2]) : [i0] "v"(wf), [i1] "a"(e.tok[4][0][1]), [i2] "v"(e.laddr[1][2]), [i3] "v"(e.acc[2][2][2]), [i4] "v"(sc.zr[2]));
  asm volatile("v_mfma_f32_16x16x32_bf16 %[o0], %[i0], %[i1], %[o0]\n\t" "v_mul_f32 %[o1], %[i2], %[i3]\n\t" "v_mul_f32 %[o2], %[i4], %[i5]\n\t" "v_mul_f32 %[o3], %[i6], %[i7]\n\t" : [o0] "+v"(e.acc[2][5]), [o1] "=&v"(tz[1][2]), [o2] "=&v"(tz[0][3]), [o3] "=&v"(tz[1][3]) : [i0] "v"(wf), [i1] "a"(e.tok[4][0][2]), [i2] "v"(e.acc[3][2][2]), [i3] "v"(sc.zr[3]), [i4] "v"(e.acc[2][2][3]), [i5] "v"(sc.zr[2]), [i6] "v"(e.acc[3][2][3]), [i7] "v"(sc.zr[3]));
  asm volatile("v_mfma_f32_16x16x32_bf16 %[o0], %[i0], %[i1], %[o0]\n\t" "v_exp_f32 %[o1], %[o1]\n\t" "v_exp_f32 %[o2], %[o2]\n\t" : [o0] "+v"(e.acc[3][5]), [o1] "+v"(tz[0][0]), [o2] "+v"(tz[1][0]) : [i0] "v"(wf), [i1] "a"(e.tok[4][0][3]));
  }
  asm volatile("s_waitcnt lgkmcnt(4)");
  { const u32x4 wf = __builtin_shufflevector(e.wlo[0], e.whi[0], 0, 1, 2, 3);
  asm volatile("v_mfma_f32_16x16x32_bf16 %[o0], %[i0], %[i1], %[o0]\n\t" "ds_read_b64_tr_b16 %[o1], %[i2] offset:40960\n\t" "v_exp_f32 %[o2], %[o2]\n\t" : [o0] "+v"(e.acc[0][4]), [o1] "=&v"(e.wlo[3]), [o2] "+v"(tz[0][1]) : [i0] "v"(wf), [i1] "a"(e.tok[5][0][0]), [i2] "v"(e.laddr[1][2]));
  asm volatile("v_mfma_f32_16x16x32_bf16 %[o0], %[i0], %[i1], %[o0]\n\t" "ds_read_b64_tr_b16 %[o1], %[i2] offset:45056\n\t" "v_exp_f32 %[o2], %[o2]\n\t" : [o0] "+v"(e.acc[1][4]), [o1] "=&v"(e.whi[3]), [o2] "+v"(tz[1][1]) : [i0] "v"(wf), [i1] "a"(e.tok[5][0][1]), [i2] "v"(e.laddr[1][2]));
  asm volatile("v_mfma_f32_16x16x32_bf16 %[o0], %[i0], %[i1], %[o0]\n\t" "v_exp_f32 %[o1], %[o1]\n\t" "v_exp_f32 %[o2], %[o2]\n\t" "v_exp_f32 %[o3], %[o3]\n\t" : [o0] "+v"(e.acc[2][4]), [o1] "+v"(tz[0][2]), [o2] "+v"(tz[1][2]), [o3] "+v"(tz[0][3]) : [i0] "v"(wf), [i1] "a"(e.tok[5][0][2]));
  asm volatile("v_mfma_f32_16x16x32_bf16 %[o0], %[i0], %[i1], %[o0]\n\t" "v_exp_f32 %[o1], %[o1]\n\t" "v_fma_f32 %[o2], %[o2], %[i2], %[i2]\n\t" : [o0] "+v"(e.acc[3][4]), [o1] "+v"(tz[1][3]), [o2] "+v"(tz[0][0]) : [i0] "v"(wf), [i1] "a"(e.tok[5][0][3]), [i2] "v"(sc.ri[2]));
  }
  asm volatile("s_waitcnt lgkmcnt(4)");
  { const u32x4 wf = __builtin_shufflevector(e.wlo[1], e.whi[1], 0, 1, 2, 3);
  asm volatile("v_mfma_f32_16x16x32_bf16 %[o0], %[i0], %[i1], %[o0]\n\t" "ds_read_b64_tr_b16 %[o1], %[i2] offset:49152\n\t" "v_fma_f32 %[o2], %[o2], %[i3], %[i3]\n\t" : [o0] "+v"(e.acc[0][5]), [o1] "=&v"(e.wlo[0]), [o2] "+v"(tz[1][0]) : [i0] "v"(wf), [i1] "a"(e.tok[5][0][0]), [i2] "v"(e.laddr[1][2]), [i3] "v"(sc.ri[3]));
  asm volatile("v_mfma_f32_16x16x32_bf16 %[o0], %[i0], %[i1], %[o0]\n\t" "ds_read_b64_tr_b16 %[o1], %[i2] offset:53248\n\t" "v_fma_f32 %[o2], %[o2], %[i3], %[i3]\n\t" : [o0] "+v"(e.acc[1][5]), [o1] "=&v"(e.whi[0]), [o2] "+v"(tz[0][1]) : [i0] "v"(wf), [i1] "a"(e.tok[5][0][1]), [i2] "v"(e.laddr[1][2]), [i3] "v"(sc.ri[2]));
  asm volatile("v_mfma_f32_16x16x32_bf16 %[o0], %[i0], %[i1], %[o0]\n\t" "v_fma_f32 %[o1], %[o1], %[i2], %[i2]\n\t" "v_fma_f32 %[o2], %[o2], %[i3], %[i3]\n\t" "v_fma_f32 %[o3], %[o3], %[i4], %[i4]\n\t" : [o0] "+v"(e.acc[2][5]), [o1] "+v"(tz[1][1]), [o2] "+v"(tz[0][2]), [o3] "+v"(tz[1][2]) : [i0] "v"(wf), [i1] "a"(e.tok[5][0][2]), [i2] "v"(sc.ri[3]), [i3] "v"(sc.ri[2]), [i4] "v"(sc.ri[3]));
  asm volatile("v_mfma_f32_16x16x32_bf16 %[o0], %[i0], %[i1], %[o0]\n\t" "v_fma_f32 %[o1], %[o1], %[i2], %[i2]\n\t" "v_fma_f32 %[o2], %[o2], %[i3], %[i3]\n\t" : [o0] "+v"(e.acc[3][5]), [o1] "+v"(tz[0][3]), [o2] "+v"(tz[1][3]) : [i0] "v"(wf), [i1] "a"(e.tok[5][0][3]), [i2] "v"(sc.ri[2]), [i3] "v"(sc.ri[3]));
  }
  asm volatile("s_waitcnt lgkmcnt(4)");
  { const u32x4 wf = __builtin_shufflevector(e.wlo[2], e.whi[2], 0, 1, 2, 3);
  asm volatile("v_mfma_f32_16x16x32_bf16 %[o0], %[i0], %[i1], %[o0]\n\t" "ds_read_b64_tr_b16 %[o1], %[i2] offset:57344\n\t" "v_rcp_f32 %[o2], %[o2]\n\t" : [o0] "+v"(e.acc[0][4]), [o1] "=&v"(e.wlo[1]), [o2] "+v"(tz[0][0]) : [i0] "v"(wf), [i1] "a"(e.tok[6][0][0]), [i2] "v"(e.laddr[1][2]));
  asm volatile("v_mfma_f32_16x16x32_bf16 %[o0], %[i0], %[i1], %[o0]\n\t" "ds_read_b64_tr_b16 %[o1], %[i2] offset:61440\n\t" "v_rcp_f32 %[o2], %[o2]\n\t" : [o0] "+v"(e.acc[1][4]), [o1] "=&v"(e.whi[1]), [o2] "+v"(tz[1][0]) : [i0] "v"(wf), [i1] "a"(e.tok[6][0][1]), [i2] "v"(e.laddr[1][2]));
  asm volatile("v_mfma_f32_16x16x32_bf16 %[o0], %[i0], %[i1], %[o0]\n\t" "v_rcp_f32 %[o1], %[o1]\n\t" "v_rcp_f32 %[o2], %[o2]\n\t" "v_rcp_f32 %[o3], %[o3]\n\t" : [o0] "+v"(e.acc[2][4]), [o1] "+v"(tz[0][1]), [o2] "+v"(tz[1][1]), [o3] "+v"(tz[0][2]) : [i0] "v"(wf), [i1] "a"(e.tok[6][0][2]));
  asm volatile("v_mfma_f32_16x16x32_bf16 %[o0], %[i0], %[i1], %[o0]\n\t" "v_rcp_f32 %[o1], %[o1]\n\t" "v_rcp_f32 %[o2], %[o2]\n\t" : [o0] "+v"(e.acc[3][4]), [o1] "+v"(tz[1][2]), [o2] "+v"(tz[0][3]) : [i0] "v"(wf), [i1] "a"(e.tok[6][0][3]));
  }
  asm volatile("s_waitcnt lgkmcnt(4)");
  { const u32x4 wf = __builtin_shufflevector(e.wlo[3], e.whi[3], 0, 1, 2, 3);
  asm volatile("v_mfma_f32_16x16x32_bf16 %[o0], %[i0], %[i1], %[o0]\n\t" "ds_read_b64_tr_b16 %[o1], %[i2] offset:16384\n\t" "v_rcp_f32 %[o2], %[o2]\n\t" : [o0] "+v"(e.acc[0][5]), [o1] "=&v"(e.wlo[2]), [o2] "+v"(tz[1][3]) : [i0] "v"(wf), [i1] "a"(e.tok[6][0][0]), [i2] "v"(e.laddr[0][3]));
  asm volatile("v_mfma_f32_16x16x32_bf16 %[o0], %[i0], %[i1], %[o0]\n\t" "ds_read_b64_tr_b16 %[o1], %[i2] offset:20480\n\t" "v_mul_f32 %[o2], %[i3], %[i4]\n\t" : [o0] "+v"(e.acc[1][5]), [o1] "=&v"(e.whi[2]), [o2] "=&v"(tw[0][0]) : [i0] "v"(wf), [i1] "a"(e.tok[6][0][1]), [i2] "v"(e.laddr[0][3]), [i3] "v"(e.acc[2][3][0]), [i4] "v"(tz[0][0]));
  asm volatile("v_mfma_f32_16x16x32_bf16 %[o0], %[i0], %[i1], %[o0]\n\t" "v_mul_f32 %[o1], %[i2], %[i3]\n\t" "v_mul_f32 %[o2], %[i4], %[i5]\n\t" "v_mul_f32 %[o3], %[i6], %[i7]\n\t" : [o0] "+v"(e.acc[2][5]), [o1] "=&v"(tw[1][0]), [o2] "=&v"(tw[0][1]), [o3] "=&v"(tw[1][1]) : [i0] "v"(wf), [i1] "a"(e.tok[6][0][2]), [i2] "v"(e.acc[3][3][0]), [i3] "v"(tz[1][0]), [i4] "v"(e.acc[2][3][1]), [i5] "v"(tz[0][1]), [i6] "v"(e.acc[3][3][1]), [i7] "v"(tz[1][1]));
  asm volatile("v_mfma_f32_16x16x32_bf16 %[o0], %[i0], %[i1], %[o0]\n\t" "v_mul_f32 %[o1], %[i2], %[i3]\n\t" "v_mul_f32 %[o2], %[i4], %[i5]\n\t" : [o0] "+v"(e.acc[3][5]), [o1] "=&v"(tw[0][2]), [o2] "=&v"(tw[1][2]) : [i0] "v"(wf), [i1] "a"(e.tok[6][0][3]), [i2] "v"(e.acc[2][3][2]), [i3] "v"(tz[0][2]), [i4] "v"(e.acc[3][3][2]), [i5] "v"(tz[1][2]));
  }
  asm volatile("s_waitcnt lgkmcnt(4)");
  { const u32x4 wf = __builtin_shufflevector(e.wlo[0], e.whi[0], 0, 1, 2, 3);
  asm volatile("v_mfma_f32_16x16x32_bf16 %[o0], %[i0], %[i1], %[o0]\n\t" "ds_read_b64_tr_b16 %[o1], %[i2] offset:24576\n\t" "v_mul_f32 %[o2], %[i3], %[i4]\n\t" : [o0] "+v"(e.acc[0][4]), [o1] "=&v"(e.wlo[3]), [o2] "=&v"(tw[0][3]) : [i0] "v"(wf), [i1] "a"(e.tok[7][0][0]), [i2] "v"(e.laddr[0][3]), [i3] "v"(e.acc[2][3][3]), [i4] "v"(tz[0][3]));
  asm volatile("v_mfma_f32_16x16x32_bf16 %[o0], %[i0], %[i1], %[o0]\n\t" "ds_read_b64_tr_b16 %[o1], %[i2] offset:28672\n\t" "v_mul_f32 %[o2], %[i3], %[i4]\n\t" : [o0] "+v"(e.acc[1][4]), [o1] "=&v"(e.whi[3]), [o2] "=&v"(tw[1][3]) : [i0] "v"(wf), [i1] "a"(e.tok[7][0][1]), [i2] "v"(e.laddr[0][3]), [i3] "v"(e.acc[3][3][3]), [i4] "v"(tz[1][3]));
  asm volatile("v_mfma_f32_16x16x32_bf16 %[o0], %[i0], %[i1], %[o0]\n\t" "v_cvt_pk_bf16_f32 %[o1], %[i2], %[i3]\n\t" "v_cvt_pk_bf16_f32 %[o2], %[i4], %[i5]\n\t" "v_cvt_pk_bf16_f32 %[o3], %[i6], %[i7]\n\t" : [o0] "+v"(e.acc[2][4]), [o1] "=&v"(pk[1][2][0]), [o2] "=&v"(pk[1][3][0]), [o3] "=&v"(pk[1][2][1]) : [i0] "v"(wf), [i1] "a"(e.tok[7][0][2]), [i2] "v"(tw[0][0]), [i3] "v"(tw[0][1]), [i4] "v"(tw[1][0]), [i5] "v"(tw[1][1]), [i6] "v"(tw[0][2]), [i7] "v"(tw[0][3]));
  asm volatile("v_mfma_f32_16x16x32_bf16 %[o0], %[i0], %[i1], %[o0]\n\t" "v_cvt_pk_bf16_f32 %[o1], %[i2], %[i3]\n\t" : [o0] "+v"(e.acc[3][4]), [o1] "=&v"(pk[1][3][1]) : [i0] "v"(wf), [i1] "a"(e.tok[7][0][3]), [i2] "v"(tw[1][2]), [i3] "v"(tw[1][3]));
  st128a(c.wsr, p0[1] + 0, so.pair[1], (u32x4){pk[0][2][0], pk[0][2][1], pk[1][2][0], pk[1][2][1]});
  }
  asm volatile("s_waitcnt lgkmcnt(4)");
  { const u32x4 wf = __builtin_shufflevector(e.wlo[1], e.whi[1], 0, 1, 2, 3);
  asm volatile("v_mfma_f32_16x16x32_bf16 %[o0], %[i0], %[i1], %[o0]\n\t" "ds_read_b64_tr_b16 %[o1], %[i2] offset:32768\n\t" : [o0] "+v"(e.acc[0][5]), [o1] "=&v"(e.wlo[0]) : [i0] "v"(wf), [i1] "a"(e.tok[7][0][0]), [i2] "v"(e.laddr[0][3]));
  st128a(c.wsr, p0[1] + 1024, so.pair[1], (u32x4){pk[0][3][0], pk[0][3][1], pk[1][3][0], pk[1][3][1]});
  asm volatile("v_mfma_f32_16x16x32_bf16 %[o0], %[i0], %[i1], %[o0]\n\t" "ds_read_b64_tr_b16 %[o1], %[i2] offset:36864\n\t" : [o0] "+v"(e.acc[1][5]), [o1] "=&v"(e.whi[0]) : [i0] "v"(wf), [i1] "a"(e.tok[7][0][1]), [i2] "v"(e.laddr[0][3]));
  asm volatile("v_mfma_f32_16x16x32_bf16 %[o0], %[i0], %[i1], %[o0]\n\t" : [o0] "+v"(e.acc[2][5]) : [i0] "v"(wf), [i1] "a"(e.tok[7][0][2]));
  asm volatile("v_mfma_f32_16x16x32_bf16 %[o0], %[i0], %[i1], %[o0]\n\t" : [o0] "+v"(e.acc[3][5]) : [i0] "v"(wf), [i1] "a"(e.tok[7][0][3]));
  }
  asm volatile("s_waitcnt lgkmcnt(4)");
  { const u32x4 wf = __builtin_shufflevector(e.wlo[2], e.whi[2], 0, 1, 2, 3);
  asm volatile("v_mfma_f32_16x16x32_bf16 %[o0], %[i0], %[i1], %[o0]\n\t" "ds_read_b64_tr_b16 %[o1], %[i2] offset:40960\n\t" "v_mul_f32 %[o2], %[i3], %[i4]\n\t" : [o0] "+v"(e.acc[0][6]), [o1] "=&v"(e.wlo[1]), [o2] "=&v"(tz[0][0]) : [i0] "v"(wf), [i1] "a"(e.tok[1][0][0]), [i2] "v"(e.laddr[0][3]), [i3] "v"(e.acc[0][4][0]), [i4] "v"(sc.zr[0]));
  asm volatile("v_mfma_f32_16x16x32_bf16 %[o0], %[i0], %[i1], %[o0]\n\t" "ds_read_b64_tr_b16 %[o1], %[i2] offset:45056\n\t" "v_mul_f32 %[o2], %[i3], %[i4]\n\t" : [o0] "+v"(e.acc[1][6]), [o1] "=&v"(e.whi[1]), [o2] "=&v"(tz[1][0]) : [i0] "v"(wf), [i1] "a"(e.tok[1][0][1]), [i2] "v"(e.laddr[0][3]), [i3] "v"(e.acc[1][4][0]), [i4] "v"(sc.zr[1]));
  asm volatile("v_mfma_f32_16x16x32_bf16 %[o0], %[i0], %[i1], %[o0]\n\t" "v_mul_f32 %[o1], %[i2], %[i3]\n\t" "v_mul_f32 %[o2], %[i4], %[i5]\n\t" "v_mul_f32 %[o3], %[i6], %[i7]\n\t" : [o0] "+v"(e.acc[2][6]), [o1] "=&v"(tz[0][1]), [o2] "=&v"(tz[1][1]), [o3] "=&v"(tz[0][2]) : [i0] "v"(wf), [i1] "a"(e.tok[1][0][2]), [i2] "v"(e.acc[0][4][1]), [i3] "v"(sc.zr[0]), [i4] "v"(e.acc[1][4][1]), [i5] "v"(sc.zr[1]), [i6] "v"(e.acc[0][4][2]), [i7] "v"(sc.zr[0]));
  asm volatile("v_mfma_f32_16x16x32_bf16 %[o0], %[i0], %[i1], %[o0]\n\t" "v_mul_f32 %[o1], %[i2], %[i3]\n\t" "v_mul_f32 %[o2], %[i4], %[i5]\n\t" : [o0] "+v"(e.acc[3][6]), [o1] "=&v"(tz[1][2]), [o2] "=&v"(tz[0][3]) : [i0] "v"(wf), [i1] "a"(e.tok[1][0][3]), [i2] "v"(e.acc[1][4][2]), [i3] "v"(sc.zr[1]), [i4] "v"(e.acc[0][4][3]), [i5] "v"(sc.zr[0]));
  }
  asm volatile("s_waitcnt lgkmcnt(4)");
  { const u32x4 wf = __builtin_shufflevector(e.wlo[3], e.whi[3], 0, 1, 2, 3);
  asm volatile("v_mfma_f32_16x16x32_bf16 %[o0], %[i0], %[i1], %[o0]\n\t" "ds_read_b64_tr_b16 %[o1], %[i2] offset:49152\n\t" "v_mul_f32 %[o2], %[i3], %[i4]\n\t" : [o0] "+v"(e.acc[0][7]), [o1] "=&v"(e.wlo[2]), [o2] "=&v"(tz[1][3]) : [i0] "v"(wf), [i1] "a"(e.tok[1][0][0]), [i2] "v"(e.laddr[0][3]), [i3] "v"(e.acc[1][4][3]), [i4] "v"(sc.zr[1]));
  asm volatile("v_mfma_f32_16x16x32_bf16 %[o0], %[i0], %[i1], %[o0]\n\t" "ds_read_b64_tr_b16 %[o1], %[i2] offset:53248\n\t" "v_exp_f32 %[o2], %[o2]\n\t" : [o0] "+v"(e.acc[1][7]), [o1] "=&v"(e.whi[2]), [o2] "+v"(tz[0][0]) : [i0] "v"(wf), [i1] "a"(e.tok[1][0][1]), [i2] "v"(e.laddr[0][3]));
  asm volatile("v_mfma_f32_16x16x32_bf16 %[o0], %[i0], %[i1], %[o0]\n\t" "v_exp_f32 %[o1], %[o1]\n\t" "v_exp_f32 %[o2], %[o2]\n\t" "v_exp_f32 %[o3], %[o3]\n\t" : [o0] "+v"(e.acc[2][7]), [o1] "+v"(tz[1][0]), [o2] "+v"(tz[0][1]), [o3] "+v"(tz[1][1]) : [i0] "v"(wf), [i1] "a"(e.tok[1][0][2]));
  asm volatile("v_mfma_f32_16x16x32_bf16 %[o0], %[i0], %[i1], %[o0]\n\t" "v_exp_f32 %[o1], %[o1]\n\t" "v_exp_f32 %[o2], %[o2]\n\t" : [o0] "+v"(e.acc[3][7]), [o1] "+v"(tz[0][2]), [o2] "+v"(tz[1][2]) : [i0] "v"(wf), [i1] "a"(e.tok[1][0][3]));
  }
  asm volatile("s_waitcnt lgkmcnt(4)");
  { const u32x4 wf = __builtin_shufflevector(e.wlo[0], e.whi[0], 0, 1, 2, 3);
  asm volatile("v_mfma_f32_16x16x32_bf16 %[o0], %[i0], %[i1], %[o0]\n\t" "ds_read_b64_tr_b16 %[o1], %[i2] offset:57344\n\t" "v_exp_f32 %[o2], %[o2]\n\t" : [o0] "+v"(e.acc[0][6]), [o1] "=&v"(e.wlo[3]), [o2] "+v"(tz[0][3]) : [i0] "v"(wf), [i1] "a"(e.tok[2][0][0]), [i2] "v"(e.laddr[0][3]));
  asm volatile("v_mfma_f32_16x16x32_bf16 %[o0], %[i0], %[i1], %[o0]\n\t" "ds_read_b64_tr_b16 %[o1], %[i2] offset:61440\n\t" "v_exp_f32 %[o2], %[o2]\n\t" : [o0] "+v"(e.acc[1][6]), [o1] "=&v"(e.whi[3]), [o2] "+v"(tz[1][3]) : [i0] "v"(wf), [i1] "a"(e.tok[2][0][1]), [i2] "v"(e.laddr[0][3]));
  asm volatile("v_mfma_f32_16x16x32_bf16 %[o0], %[i0], %[i1], %[o0]\n\t" "v_fma_f32 %[o1], %[o1], %[i2], %[i2]\n\t" "v_fma_f32 %[o2], %[o2], %[i3], %[i3]\n\t" "v_fma_f32 %[o3], %[o3], %[i4], %[i4]\n\t" : [o0] "+v"(e.acc[2][6]), [o1] "+v"(tz[0][0]), [o2] "+v"(tz[1][0]), [o3] "+v"(tz[0][1]) : [i0] "v"(wf), [i1] "a"(e.tok[2][0][2]), [i2] "v"(sc.ri[0]), [i3] "v"(sc.ri[1]), [i4] "v"(sc.ri[0]));
  asm volatile("v_mfma_f32_16x16x32_bf16 %[o0], %[i0], %[i1], %[o0]\n\t" "v_fma_f32 %[o1], %[o1], %[i2], %[i2]\n\t" "v_fma_f32 %[o2], %[o2], %[i3], %[i3]\n\t" : [o0] "+v"(e.acc[3][6]), [o1] "+v"(tz[1][1]), [o2] "+v"(tz[0][2]) : [i0] "v"(wf), [i1] "a"(e.tok[2][0][3]), [i2] "v"(sc.ri[1]), [i3] "v"(sc.ri[0]));
  }
  asm volatile("s_waitcnt lgkmcnt(4)");
  { const u32x4 wf = __builtin_shufflevector(e.wlo[1], e.whi[1], 0, 1, 2, 3);
  asm volatile("v_mfma_f32_16x16x32_bf16 %[o0], %[i0], %[i1], %[o0]\n\t" "ds_read_b64_tr_b16 %[o1], %[i2] offset:0\n\t" "v_fma_f32 %[o2], %[o2], %[i3], %[i3]\n\t" : [o0] "+v"(e.acc[0][7]), [o1] "=&v"(e.wlo[0]), [o2] "+v"(tz[1][2]) : [i0] "v"(wf), [i1] "a"(e.tok[2][0][0]), [i2] "v"(e.laddr[1][3]), [i3] "v"(sc.ri[1]));
  asm volatile("v_mfma_f32_16x16x32_bf16 %[o0], %[i0], %[i1], %[o0]\n\t" "ds_read_b64_tr_b16 %[o1], %[i2] offset:4096\n\t" "v_fma_f32 %[o2], %[o2], %[i3], %[i3]\n\t" : [o0] "+v"(e.acc[1][7]), [o1] "=&v"(e.whi[0]), [o2] "+v"(tz[0][3]) : [i0] "v"(wf), [i1] "a"(e.tok[2][0][1]), [i2] "v"(e.laddr[1][3]), [i3] "v"(sc.ri[0]));
  asm volatile("v_mfma_f32_16x16x32_bf16 %[o0], %[i0], %[i1], %[o0]\n\t" "v_fma_f32 %[o1], %[o1], %[i2], %[i2]\n\t" "v_rcp_f32 %[o2], %[o2]\n\t" "v_rcp_f32 %[o3], %[o3]\n\t" : [o0] "+v"(e.acc[2][7]), [o1] "+v"(tz[1][3]), [o2] "+v"(tz[0][0]), [o3] "+v"(tz[1][0]) : [i0] "v"(wf), [i1] "a"(e.tok[2][0][2]), [i2] "v"(sc.ri[1]));
  asm volatile("v_mfma_f32_16x16x32_bf16 %[o0], %[i0], %[i1], %[o0]\n\t" "v_rcp_f32 %[o1], %[o1]\n\t" "v_rcp_f32 %[o2], %[o2]\n\t" : [o0] "+v"(e.acc[3][7]), [o1] "+v"(tz[0][1]), [o2] "+v"(tz[1][1]) : [i0] "v"(wf), [i1] "a"(e.tok[2][0][3]));
  }
  asm volatile("s_waitcnt lgkmcnt(4)");
  { const u32x4 wf = __builtin_shufflevector(e.wlo[2], e.whi[2], 0, 1, 2, 3);
  asm volatile("v_mfma_f32_16x16x32_bf16 %[o0], %[i0], %[i1], %[o0]\n\t" "ds_read_b64_tr_b16 %[o1], %[i2] offset:8192\n\t" "v_rcp_f32 %[o2], %[o2]\n\t" : [o0] "+v"(e.acc[0][6]), [o1] "=&v"(e.wlo[1]), [o2] "+v"(tz[0][2]) : [i0] "v"(wf), [i1] "a"(e.tok[3][0][0]), [i2] "v"(e.laddr[1][3]));
  asm volatile("v_mfma_f32_16x16x32_bf16 %[o0], %[i0], %[i1], %[o0]\n\t" "ds_read_b64_tr_b16 %[o1], %[i2] offset:12288\n\t" "v_rcp_f32 %[o2], %[o2]\n\t" : [o0] "+v"(e.acc[1][6]), [o1] "=&v"(e.whi[1]), [o2] "+v"(tz[1][2]) : [i0] "v"(wf), [i1] "a"(e.tok[3][0][1]), [i2] "v"(e.laddr[1][3]));
  asm volatile("v_mfma_f32_16x16x32_bf16 %[o0], %[i0], %[i1], %[o0]\n\t" "v_rcp_f32 %[o1], %[o1]\n\t" "v_rcp_f32 %[o2], %[o2]\n\t" "v_mul_f32 %[o3], %[i2], %[i3]\n\t" : [o0] "+v"(e.acc[2][6]), [o1] "+v"(tz[0][3]), [o2] "+v"(tz[1][3]), [o3] "=&v"(tw[0][0]) : [i0] "v"(wf), [i1] "a"(e.tok[3][0][2]), [i2] "v"(e.acc[0][5][0]), [i3] "v"(tz[0][0]));
  asm volatile("v_mfma_f32_16x16x32_bf16 %[o0], %[i0], %[i1], %[o0]\n\t" "v_mul_f32 %[o1], %[i2], %[i3]\n\t" "v_mul_f32 %[o2], %[i4], %[i5]\n\t" : [o0] "+v"(e.acc[3][6]), [o1] "=&v"(tw[1][0]), [o2] "=&v"(tw[0][1]) : [i0] "v"(wf), [i1] "a"(e.tok[3][0][3]), [i2] "v"(e.acc[1][5][0]), [i3] "v"(tz[1][0]), [i4] "v"(e.acc[0][5][1]), [i5] "v"(tz[0][1]));
  }
  asm volatile("s_waitcnt lgkmcnt(4)");
  { const u32x4 wf = __builtin_shufflevector(e.wlo[3], e.whi[3], 0, 1, 2, 3);
  asm volatile("v_mfma_f32_16x16x32_bf16 %[o0], %[i0], %[i1], %[o0]\n\t" "ds_read_b64_tr_b16 %[o1], %[i2] offset:16384\n\t" "v_mul_f32 %[o2], %[i3], %[i4]\n\t" : [o0] "+v"(e.acc[0][7]), [o1] "=&v"(e.wlo[2]), [o2] "=&v"(tw[1][1]) : [i0] "v"(wf), [i1] "a"(e.tok[3][0][0]), [i2] "v"(e.laddr[1][3]), [i3] "v"(e.acc[1][5][1]), [i4] "v"(tz[1][1]));
  asm volatile("v_mfma_f32_16x16x32_bf16 %[o0], %[i0], %[i1], %[o0]\n\t" "ds_read_b64_tr_b16 %[o1], %[i2] offset:20480\n\t" "v_mul_f32 %[o2], %[i3], %[i4]\n\t" : [o0] "+v"(e.acc[1][7]), [o1] "=&v"(e.whi[2]), [o2] "=&v"(tw[0][2]) : [i0] "v"(wf), [i1] "a"(e.tok[3][0][1]), [i2] "v"(e.laddr[1][3]), [i3] "v"(e.acc[0][5][2]), [i4] "v"(tz[0][2]));
  asm volatile("v_mfma_f32_16x16x32_bf16 %[o0], %[i0], %[i1], %[o0]\n\t" "v_mul_f32 %[o1], %[i2], %[i3]\n\t" "v_mul_f32 %[o2], %[i4], %[i5]\n\t" "v_mul_f32 %[o3], %[i6], %[i7]\n\t" : [o0] "+v"(e.acc[2][7]), [o1] "=&v"(tw[1][2]), [o2] "=&v"(tw[0][3]), [o3] "=&v"(tw[1][3]) : [i0] "v"(wf), [i1] "a"(e.tok[3][0][2]), [i2] "v"(e.acc[1][5][2]), [i3] "v"(tz[1][2]), [i4] "v"(e.acc[0][5][3]), [i5] "v"(tz[0][3]), [i6] "v"(e.acc[1][5][3]), [i7] "v"(tz[1][3]));
  asm volatile("v_mfma_f32_16x16x32_bf16 %[o0], %[i0], %[i1], %[o0]\n\t" "v_cvt_pk_bf16_f32 %[o1], %[i2], %[i3]\n\t" "v_cvt_pk_bf16_f32 %[o2], %[i4], %[i5]\n\t" : [o0] "+v"(e.acc[3][7]), [o1] "=&v"(pk[0][0][0]), [o2] "=&v"(pk[0][1][0]) : [i0] "v"(wf), [i1] "a"(e.tok[3][0][3]), [i2] "v"(tw[0][0]), [i3] "v"(tw[0][1]), [i4] "v"(tw[1][0]), [i5] "v"(tw[1][1]));
  }
  asm volatile("s_waitcnt lgkmcnt(4)");
  { const u32x4 wf = __builtin_shufflevector(e.wlo[0], e.whi[0], 0, 1, 2, 3);
  asm volatile("v_mfma_f32_16x16x32_bf16 %[o0], %[i0], %[i1], %[o0]\n\t" "ds_read_b64_tr_b16 %[o1], %[i2] offset:24576\n\t" "v_cvt_pk_bf16_f32 %[o2], %[i3], %[i4]\n\t" : [o0] "+v"(e.acc[0][6]), [o1] "=&v"(e.wlo[3]), [o2] "=&v"(pk[0][0][1]) : [i0] "v"(wf), [i1] "a"(e.tok[4][0][0]), [i2] "v"(e.laddr[1][3]), [i3] "v"(tw[0][2]), [i4] "v"(tw[0][3]));
  asm volatile("v_mfma_f32_16x16x32_bf16 %[o0], %[i0], %[i1], %[o0]\n\t" "ds_read_b64_tr_b16 %[o1], %[i2] offset:28672\n\t" "v_cvt_pk_bf16_f32 %[o2], %[i3], %[i4]\n\t" : [o0] "+v"(e.acc[1][6]), [o1] "=&v"(e.whi[3]), [o2] "=&v"(pk[0][1][1]) : [i0] "v"(wf), [i1] "a"(e.tok[4][0][1]), [i2] "v"(e.laddr[1][3]), [i3] "v"(tw[1][2]), [i4] "v"(tw[1][3]));
  asm volatile("v_mfma_f32_16x16x32_bf16 %[o0], %[i0], %[i1], %[o0]\n\t" : [o0] "+v"(e.acc[2][6]) : [i0] "v"(wf), [i1] "a"(e.tok[4][0][2]));
  st128a(c.wsr, p0[0] + 0, so.pair[2], (u32x4){pk[1][0][0], pk[1][0][1], pk[0][0][0], pk[0][0][1]});
  st128a(c.wsr, p0[0] + 1024, so.pair[2], (u32x4){pk[1][1][0], pk[1][1][1], pk[0][1][0], pk[0][1][1]});
  asm volatile("v_mul_f32 %[o0], %[i0], %[i1]\n\t" : [o0] "=&v"(tz[0][0]) : [i0] "v"(e.acc[2][4][0]), [i1] "v"(sc.zr[2]));
  asm volatile("v_mfma_f32_16x16x32_bf16 %[o0], %[i0], %[i1], %[o0]\n\t" "v_mul_f32 %[o1], %[i2], %[i3]\n\t" "v_mul_f32 %[o2], %[i4], %[i5]\n\t" : [o0] "+v"(e.acc[3][6]), [o1] "=&v"(tz[1][0]), [o2] "=&v"(tz[0][1]) : [i0] "v"(wf), [i1] "a"(e.tok[4][0][3]), [i2] "v"(e.acc[3][4][0]), [i3] "v"(sc.zr[3]), [i4] "v"(e.acc[2][4][1]), [i5] "v"(sc.zr[2]));
  }
  asm volatile("s_waitcnt lgkmcnt(4)");
  { const u32x4 wf = __builtin_shufflevector(e.wlo[1], e.whi[1], 0, 1, 2, 3);
  asm volatile("v_mfma_f32_16x16x32_bf16 %[o0], %[i0], %[i1], %[o0]\n\t" "ds_read_b64_tr_b16 %[o1], %[i2] offset:32768\n\t" "v_mul_f32 %[o2], %[i3], %[i4]\n\t" : [o0] "+v"(e.acc[0][7]), [o1] "=&v"(e.wlo[0]), [o2] "=&v"(tz[1][1]) : [i0] "v"(wf), [i1] "a"(e.tok[4][0][0]), [i2] "v"(e.laddr[1][3]), [i3] "v"(e.acc[3][4][1]), [i4] "v"(sc.zr[3]));
  asm volatile("v_mfma_f32_16x16x32_bf16 %[o0], %[i0], %[i1], %[o0]\n\t" "ds_read_b64_tr_b16 %[o1], %[i2] offset:36864\n\t" "v_mul_f32 %[o2], %[i3], %[i4]\n\t" : [o0] "+v"(e.acc[1][7]), [o1] "=&v"(e.whi[0]), [o2] "=&v"(tz[0][2]) : [i0] "v"(wf), [i1] "a"(e.tok[4][0][1]), [i2] "v"(e.laddr[1][3]), [i3] "v"(e.acc[2][4][2]), [i4] "v"(sc.zr[2]));
  asm volatile("v_mfma_f32_16x16x32_bf16 %[o0], %[i0], %[i1], %[o0]\n\t" "v_mul_f32 %[o1], %[i2], %[i3]\n\t" "v_mul_f32 %[o2], %[i4], %[i5]\n\t" "v_mul_f32 %[o3], %[i6], %[i7]\n\t" : [o0] "+v"(e.acc[2][7]), [o1] "=&v"(tz[1][2]), [o2] "=&v"(tz[0][3]), [o3] "=&v"(tz[1][3]) : [i0] "v"(wf), [i1] "a"(e.tok[4][0][2]), [i2] "v"(e.acc[3][4][2]), [i3] "v"(sc.zr[3]), [i4] "v"(e.acc[2][4][3]), [i5] "v"(sc.zr[2]), [i6] "v"(e.acc[3][4][3]), [i7] "v"(sc.zr[3]));
  asm volatile("v_mfma_f32_16x16x32_bf16 %[o0], %[i0], %[i1], %[o0]\n\t" "v_exp_f32 %[o1], %[o1]\n\t" "v_exp_f32 %[o2], %[o2]\n\t" : [o0] "+v"(e.acc[3][7]), [o1] "+v"(tz[0][0]), [o2] "+v"(tz[1][0]) : [i0] "v"(wf), [i1] "a"(e.tok[4][0][3]));
  }
  asm volatile("s_waitcnt lgkmcnt(4)");
  { const u32x4 wf = __builtin_shufflevector(e.wlo[2], e.whi[2], 0, 1, 2, 3);
  asm volatile("v_mfma_f32_16x16x32_bf16 %[o0], %[i0], %[i1], %[o0]\n\t" "ds_read_b64_tr_b16 %[o1], %[i2] offset:40960\n\t" "v_exp_f32 %[o2], %[o2]\n\t" : [o0] "+v"(e.acc[0][6]), [o1] "=&v"(e.wlo[1]), [o2] "+v"(tz[0][1]) : [i0] "v"(wf), [i1] "a"(e.tok[5][0][0]), [i2] "v"(e.laddr[1][3]));
  asm volatile("v_mfma_f32_16x16x32_bf16 %[o0], %[i0], %[i1], %[o0]\n\t" "ds_read_b64_tr_b16 %[o1], %[i2] offset:45056\n\t" "v_exp_f32 %[o2], %[o2]\n\t" : [o0] "+v"(e.acc[1][6]), [o1] "=&v"(e.whi[1]), [o2] "+v"(tz[1][1]) : [i0] "v"(wf), [i1] "a"(e.tok[5][0][1]), [i2] "v"(e.laddr[1][3]));
  asm volatile("v_mfma_f32_16x16x32_bf16 %[o0], %[i0], %[i1], %[o0]\n\t" "v_exp_f32 %[o1], %[o1]\n\t" "v_exp_f32 %[o2], %[o2]\n\t" "v_exp_f32 %[o3], %[o3]\n\t" : [o0] "+v"(e.acc[2][6]), [o1] "+v"(tz[0][2]), [o2] "+v"(tz[1][2]), [o3] "+v"(tz[0][3]) : [i0] "v"(wf), [i1] "a"(e.tok[5][0][2]));
  asm volatile("v_mfma_f32_16x16x32_bf16 %[o0], %[i0], %[i1], %[o0]\n\t" "v_exp_f32 %[o1], %[o1]\n\t" "v_fma_f32 %[o2], %[o2], %[i2], %[i2]\n\t" : [o0] "+v"(e.acc[3][6]), [o1] "+v"(tz[1][3]), [o2] "+v"(tz[0][0]) : [i0] "v"(wf), [i1] "a"(e.tok[5][0][3]), [i2] "v"(sc.ri[2]));
  }
  asm volatile("s_waitcnt lgkmcnt(4)");
  { const u32x4 wf = __builtin_shufflevector(e.wlo[3], e.whi[3], 0, 1, 2, 3);
  asm volatile("v_mfma_f32_16x16x32_bf16 %[o0], %[i0], %[i1], %[o0]\n\t" "ds_read_b64_tr_b16 %[o1], %[i2] offset:49152\n\t" "v_fma_f32 %[o2], %[o2], %[i3], %[i3]\n\t" : [o0] "+v"(e.acc[0][7]), [o1] "=&v"(e.wlo[2]), [o2] "+v"(tz[1][0]) : [i0] "v"(wf), [i1] "a"(e.tok[5][0][0]), [i2] "v"(e.laddr[1][3]), [i3] "v"(sc.ri[3]));
  asm volatile("v_mfma_f32_16x16x32_bf16 %[o0], %[i0], %[i1], %[o0]\n\t" "ds_read_b64_tr_b16 %[o1], %[i2] offset:53248\n\t" "v_fma_f32 %[o2], %[o2], %[i3], %[i3]\n\t" : [o0] "+v"(e.acc[1][7]), [o1] "=&v"(e.whi[2]), [o2] "+v"(tz[0][1]) : [i0] "v"(wf), [i1] "a"(e.tok[5][0][1]), [i2] "v"(e.laddr[1][3]), [i3] "v"(sc.ri[2]));
  asm volatile("v_mfma_f32_16x16x32_bf16 %[o0], %[i0], %[i1], %[o0]\n\t" "v_fma_f32 %[o1], %[o1], %[i2], %[i2]\n\t" "v_fma_f32 %[o2], %[o2], %[i3], %[i3]\n\t" "v_fma_f32 %[o3], %[o3], %[i4], %[i4]\n\t" : [o0] "+v"(e.acc[2][7]), [o1] "+v"(tz[1][1]), [o2] "+v"(tz[0][2]), [o3] "+v"(tz[1][2]) : [i0] "v"(wf), [i1] "a"(e.tok[5][0][2]), [i2] "v"(sc.ri[3]), [i3] "v"(sc.ri[2]), [i4] "v"(sc.ri[3]));
  asm volatile("v_mfma_f32_16x16x32_bf16 %[o0], %[i0], %[i1], %[o0]\n\t" "v_fma_f32 %[o1], %[o1], %[i2], %[i2]\n\t" "v_fma_f32 %[o2], %[o2], %[i3], %[i3]\n\t" : [o0] "+v"(e.acc[3][7]), [o1] "+v"(tz[0][3]), [o2] "+v"(tz[1][3]) : [i0] "v"(wf), [i1] "a"(e.tok[5][0][3]), [i2] "v"(sc.ri[2]), [i3] "v"(sc.ri[3]));
  }
  asm volatile("s_waitcnt lgkmcnt(4)");
  { const u32x4 wf = __builtin_shufflevector(e.wlo[0], e.whi[0], 0, 1, 2, 3);
  asm volatile("v_mfma_f32_16x16x32_bf16 %[o0], %[i0], %[i1], %[o0]\n\t" "ds_read_b64_tr_b16 %[o1], %[i2] offset:57344\n\t" "v_rcp_f32 %[o2], %[o2]\n\t" : [o0] "+v"(e.acc[0][6]), [o1] "=&v"(e.wlo[3]), [o2] "+v"(tz[0][0]) : [i0] "v"(wf), [i1] "a"(e.tok[6][0][0]), [i2] "v"(e.laddr[1][3]));
  asm volatile("v_mfma_f32_16x16x32_bf16 %[o0], %[i0], %[i1], %[o0]\n\t" "ds_read_b64_tr_b16 %[o1], %[i2] offset:61440\n\t" "v_rcp_f32 %[o2], %[o2]\n\t" : [o0] "+v"(e.acc[1][6]), [o1] "=&v"(e.whi[3]), [o2] "+v"(tz[1][0]) : [i0] "v"(wf), [i1] "a"(e.tok[6][0][1]), [i2] "v"(e.laddr[1][3]));
  asm volatile("v_mfma_f32_16x16x32_bf16 %[o0], %[i0], %[i1], %[o0]\n\t" "v_rcp_f32 %[o1], %[o1]\n\t" "v_rcp_f32 %[o2], %[o2]\n\t" "v_rcp_f32 %[o3], %[o3]\n\t" : [o0] "+v"(e.acc[2][6]), [o1] "+v"(tz[0][1]), [o2] "+v"(tz[1][1]), [o3] "+v"(tz[0][2]) : [i0] "v"(wf), [i1] "a"(e.tok[6][0][2]));
  asm volatile("v_mfma_f32_16x16x32_bf16 %[o0], %[i0], %[i1], %[o0]\n\t" "v_rcp_f32 %[o1], %[o1]\n\t" "v_rcp_f32 %[o2], %[o2]\n\t" : [o0] "+v"(e.acc[3][6]), [o1] "+v"(tz[1][2]), [o2] "+v"(tz[0][3]) : [i0] "v"(wf), [i1] "a"(e.tok[6][0][3]));
  }
  asm volatile("s_waitcnt lgkmcnt(4)");
  { const u32x4 wf = __builtin_shufflevector(e.wlo[1], e.whi[1], 0, 1, 2, 3);
  asm volatile("v_mfma_f32_16x16x32_bf16 %[o0], %[i0], %[i1], %[o0]\n\t" "ds_read_b64_tr_b16 %[o1], %[i2] offset:16384\n\t" "v_rcp_f32 %[o2], %[o2]\n\t" : [o0] "+v"(e.acc[0][7]), [o1] "=&v"(e.wlo[0]), [o2] "+v"(tz[1][3]) : [i0] "v"(wf), [i1] "a"(e.tok[6][0][0]), [i2] "v"(e.laddr[0][4]));
  asm volatile("v_mfma_f32_16x16x32_bf16 %[o0], %[i0], %[i1], %[o0]\n\t" "ds_read_b64_tr_b16 %[o1], %[i2] offset:20480\n\t" "v_mul_f32 %[o2], %[i3], %[i4]\n\t" : [o0] "+v"(e.acc[1][7]), [o1] "=&v"(e.whi[0]), [o2] "=&v"(tw[0][0]) : [i0] "v"(wf), [i1] "a"(e.tok[6][0][1]), [i2] "v"(e.laddr[0][4]), [i3] "v"(e.acc[2][5][0]), [i4] "v"(tz[0][0]));
  asm volatile("v_mfma_f32_16x16x32_bf16 %[o0], %[i0], %[i1], %[o0]\n\t" "v_mul_f32 %[o1], %[i2], %[i3]\n\t" "v_mul_f32 %[o2], %[i4], %[i5]\n\t" "v_mul_f32 %[o3], %[i6], %[i7]\n\t" : [o0] "+v"(e.acc[2][7]), [o1] "=&v"(tw[1][0]), [o2] "=&v"(tw[0][1]), [o3] "=&v"(tw[1][1]) : [i0] "v"(wf), [i1] "a"(e.tok[6][0][2]), [i2] "v"(e.acc[3][5][0]), [i3] "v"(tz[1][0]), [i4] "v"(e.acc[2][5][1]), [i5] "v"(tz[0][1]), [i6] "v"(e.acc[3][5][1]), [i7] "v"(tz[1][1]));
  asm volatile("v_mfma_f32_16x16x32_bf16 %[o0], %[i0], %[i1], %[o0]\n\t" "v_mul_f32 %[o1], %[i2], %[i3]\n\t" "v_mul_f32 %[o2], %[i4], %[i5]\n\t" : [o0] "+v"(e.acc[3][7]), [o1] "=&v"(tw[0][2]), [o2] "=&v"(tw[1][2]) : [i0] "v"(wf), [i1] "a"(e.tok[6][0][3]), [i2] "v"(e.acc[2][5][2]), [i3] "v"(tz[0][2]), [i4] "v"(e.acc[3][5][2]), [i5] "v"(tz[1][2]));
  }
  asm volatile("s_waitcnt lgkmcnt(4)");
  { const u32x4 wf = __builtin_shufflevector(e.wlo[2], e.whi[2], 0, 1, 2, 3);
  asm volatile("v_mfma_f32_16x16x32_bf16 %[o0], %[i0], %[i1], %[o0]\n\t" "ds_read_b64_tr_b16 %[o1], %[i2] offset:24576\n\t" "v_mul_f32 %[o2], %[i3], %[i4]\n\t" : [o0] "+v"(e.acc[0][6]), [o1] "=&v"(e.wlo[1]), [o2] "=&v"(tw[0][3]) : [i0] "v"(wf), [i1] "a"(e.tok[7][0][0]), [i2] "v"(e.laddr[0][4]), [i3] "v"(e.acc[2][5][3]), [i4] "v"(tz[0][3]));
  asm volatile("v_mfma_f32_16x16x32_bf16 %[o0], %[i0], %[i1], %[o0]\n\t" "ds_read_b64_tr_b16 %[o1], %[i2] offset:28672\n\t" "v_mul_f32 %[o2], %[i3], %[i4]\n\t" : [o0] "+v"(e.acc[1][6]), [o1] "=&v"(e.whi[1]), [o2] "=&v"(tw[1][3]) : [i0] "v"(wf), [i1] "a"(e.tok[7][0][1]), [i2] "v"(e.laddr[0][4]), [i3] "v"(e.acc[3][5][3]), [i4] "v"(tz[1][3]));
  asm volatile("v_mfma_f32_16x16x32_bf16 %[o0], %[i0], %[i1], %[o0]\n\t" "v_cvt_pk_bf16_f32 %[o1], %[i2], %[i3]\n\t" "v_cvt_pk_bf16_f32 %[o2], %[i4], %[i5]\n\t" "v_cvt_pk_bf16_f32 %[o3], %[i6], %[i7]\n\t" : [o0] "+v"(e.acc[2][6]), [o1] "=&v"(pk[0][2][0]), [o2] "=&v"(pk[0][3][0]), [o3] "=&v"(pk[0][2][1]) : [i0] "v"(wf), [i1] "a"(e.tok[7][0][2]), [i2] "v"(tw[0][0]), [i3] "v"(tw[0][1]), [i4] "v"(tw[1][0]), [i5] "v"(tw[1][1]), [i6] "v"(tw[0][2]), [i7] "v"(tw[0][3]));
  asm volatile("v_mfma_f32_16x16x32_bf16 %[o0], %[i0], %[i1], %[o0]\n\t" "v_cvt_pk_bf16_f32 %[o1], %[i2], %[i3]\n\t" : [o0] "+v"(e.acc[3][6]), [o1] "=&v"(pk[0][3][1]) : [i0] "v"(wf), [i1] "a"(e.tok[7][0][3]), [i2] "v"(tw[1][2]), [i3] "v"(tw[1][3]));
  st128a(c.wsr, p0[1] + 0, so.pair[2], (u32x4){pk[1][2][0], pk[1][2][1], pk[0][2][0], pk[0][2][1]});
  }
  asm volatile("s_waitcnt lgkmcnt(4)");
  { const u32x4 wf = __builtin_shufflevector(e.wlo[3], e.whi[3], 0, 1, 2, 3);
  asm volatile("v_mfma_f32_16x16x32_bf16 %[o0], %[i0], %[i1], %[o0]\n\t" "ds_read_b64_tr_b16 %[o1], %[i2] offset:32768\n\t" : [o0] "+v"(e.acc[0][7]), [o1] "=&v"(e.wlo[2]) : [i0] "v"(wf), [i1] "a"(e.tok[7][0][0]), [i2] "v"(e.laddr[0][4]));
  st128a(c.wsr, p0[1] + 1024, so.pair[2], (u32x4){pk[1][3][0], pk[1][3][1], pk[0][3][0], pk[0][3][1]});
  asm volatile("v_mfma_f32_16x16x32_bf16 %[o0], %[i0], %[i1], %[o0]\n\t" "ds_read_b64_tr_b16 %[o1], %[i2] offset:36864\n\t" : [o0] "+v"(e.acc[1][7]), [o1] "=&v"(e.whi[2]) : [i0] "v"(wf), [i1] "a"(e.tok[7][0][1]), [i2] "v"(e.laddr[0][4]));
  asm volatile("v_mfma_f32_16x16x32_bf16 %[o0], %[i0], %[i1], %[o0]\n\t" : [o0] "+v"(e.acc[2][7]) : [i0] "v"(wf), [i1] "a"(e.tok[7][0][2]));
  asm volatile("v_mfma_f32_16x16x32_bf16 %[o0], %[i0], %[i1], %[o0]\n\t" : [o0] "+v"(e.acc[3][7]) : [i0] "v"(wf), [i1] "a"(e.tok[7][0][3]));
  }
  asm volatile("s_waitcnt lgkmcnt(4)");
  { const u32x4 wf = __builtin_shufflevector(e.wlo[0], e.whi[0], 0, 1, 2, 3);
  asm volatile("v_mfma_f32_16x16x32_bf16 %[o0], %[i0], %[i1], %[o0]\n\t" "ds_read_b64_tr_b16 %[o1], %[i2] offset:40960\n\t" "v_mul_f32 %[o2], %[i3], %[i4]\n\t" : [o0] "+v"(e.acc[0][8]), [o1] "=&v"(e.wlo[3]), [o2] "=&v"(tz[0][0]) : [i0] "v"(wf), [i1] "a"(e.tok[1][0][0]), [i2] "v"(e.laddr[0][4]), [i3] "v"(e.acc[0][6][0]), [i4] "v"(sc.zr[0]));
  asm volatile("v_mfma_f32_16x16x32_bf16 %[o0], %[i0], %[i1], %[o0]\n\t" "ds_read_b64_tr_b16 %[o1], %[i2] offset:45056\n\t" "v_mul_f32 %[o2], %[i3], %[i4]\n\t" : [o0] "+v"(e.acc[1][8]), [o1] "=&v"(e.whi[3]), [o2] "=&v"(tz[1][0]) : [i0] "v"(wf), [i1] "a"(e.tok[1][0][1]), [i2] "v"(e.laddr[0][4]), [i3] "v"(e.acc[1][6][0]), [i4] "v"(sc.zr[1]));
  asm volatile("v_mfma_f32_16x16x32_bf16 %[o0], %[i0], %[i1], %[o0]\n\t" "v_mul_f32 %[o1], %[i2], %[i3]\n\t" "v_mul_f32 %[o2], %[i4], %[i5]\n\t" "v_mul_f32 %[o3], %[i6], %[i7]\n\t" : [o0] "+v"(e.acc[2][8]), [o1] "=&v"(tz[0][1]), [o2] "=&v"(tz[1][1]), [o3] "=&v"(tz[0][2]) : [i0] "v"(wf), [i1] "a"(e.tok[1][0][2]), [i2] "v"(e.acc[0][6][1]), [i3] "v"(sc.zr[0]), [i4] "v"(e.acc[1][6][1]), [i5] "v"(sc.zr[1]), [i6] "v"(e.acc[0][6][2]), [i7] "v"(sc.zr[0]));
  asm volatile("v_mfma_f32_16x16x32_bf16 %[o0], %[i0], %[i1], %[o0]\n\t" "v_mul_f32 %[o1], %[i2], %[i3]\n\t" "v_mul_f32 %[o2], %[i4], %[i5]\n\t" : [o0] "+v"(e.acc[3][8]), [o1] "=&v"(tz[1][2]), [o2] "=&v"(tz[0][3]) : [i0] "v"(wf), [i1] "a"(e.tok[1][0][3]), [i2] "v"(e.acc[1][6][2]), [i3] "v"(sc.zr[1]), [i4] "v"(e.acc[0][6][3]), [i5] "v"(sc.zr[0]));
  }
  asm volatile("s_waitcnt lgkmcnt(4)");
  { const u32x4 wf = __builtin_shufflevector(e.wlo[1], e.whi[1], 0, 1, 2, 3);
  asm volatile("v_mfma_f32_16x16x32_bf16 %[o0], %[i0], %[i1], %[o0]\n\t" "ds_read_b64_tr_b16 %[o1], %[i2] offset:49152\n\t" "v_mul_f32 %[o2], %[i3], %[i4]\n\t" : [o0] "+v"(e.acc[0][9]), [o1] "=&v"(e.wlo[0]), [o2] "=&v"(tz[1][3]) : [i0] "v"(wf), [i1] "a"(e.tok[1][0][0]), [i2] "v"(e.laddr[0][4]), [i3] "v"(e.acc[1][6][3]), [i4] "v"(sc.zr[1]));
  asm volatile("v_mfma_f32_16x16x32_bf16 %[o0], %[i0], %[i1], %[o0]\n\t" "ds_read_b64_tr_b16 %[o1], %[i2] offset:53248\n\t" "v_exp_f32 %[o2], %[o2]\n\t" : [o0] "+v"(e.acc[1][9]), [o1] "=&v"(e.whi[0]), [o2] "+v"(tz[0][0]) : [i0] "v"(wf), [i1] "a"(e.tok[1][0][1]), [i2] "v"(e.laddr[0][4]));
  asm volatile("v_mfma_f32_16x16x32_bf16 %[o0], %[i0], %[i1], %[o0]\n\t" "v_exp_f32 %[o1], %[o1]\n\t" "v_exp_f32 %[o2], %[o2]\n\t" "v_exp_f32 %[o3], %[o3]\n\t" : [o0] "+v"(e.acc[2][9]), [o1] "+v"(tz[1][0]), [o2] "+v"(tz[0][1]), [o3] "+v"(tz[1][1]) : [i0] "v"(wf), [i1] "a"(e.tok[1][0][2]));
  asm volatile("v_mfma_f32_16x16x32_bf16 %[o0], %[i0], %[i1], %[o0]\n\t" "v_exp_f32 %[o1], %[o1]\n\t" "v_exp_f32 %[o2], %[o2]\n\t" : [o0] "+v"(e.acc[3][9]), [o1] "+v"(tz[0][2]), [o2] "+v"(tz[1][2]) : [i0] "v"(wf), [i1] "a"(e.tok[1][0][3]));
  }
  asm volatile("s_waitcnt lgkmcnt(4)");
  { const u32x4 wf = __builtin_shufflevector(e.wlo[2], e.whi[2], 0, 1, 2, 3);
  asm volatile("v_mfma_f32_16x16x32_bf16 %[o0], %[i0], %[i1], %[o0]\n\t" "ds_read_b64_tr_b16 %[o1], %[i2] offset:57344\n\t" "v_exp_f32 %[o2], %[o2]\n\t" : [o0] "+v"(e.acc[0][8]), [o1] "=&v"(e.wlo[1]), [o2] "+v"(tz[0][3]) : [i0] "v"(wf), [i1] "a"(e.tok[2][0][0]), [i2] "v"(e.laddr[0][4]));
  asm volatile("v_mfma_f32_16x16x32_bf16 %[o0], %[i0], %[i1], %[o0]\n\t" "ds_read_b64_tr_b16 %[o1], %[i2] offset:61440\n\t" "v_exp_f32 %[o2], %[o2]\n\t" : [o0] "+v"(e.acc[1][8]), [o1] "=&v"(e.whi[1]), [o2] "+v"(tz[1][3]) : [i0] "v"(wf), [i1] "a"(e.tok[2][0][1]), [i2] "v"(e.laddr[0][4]));
  asm volatile("v_mfma_f32_16x16x32_bf16 %[o0], %[i0], %[i1], %[o0]\n\t" "v_fma_f32 %[o1], %[o1], %[i2], %[i2]\n\t" "v_fma_f32 %[o2], %[o2], %[i3], %[i3]\n\t" "v_fma_f32 %[o3], %[o3], %[i4], %[i4]\n\t" : [o0] "+v"(e.acc[2][8]), [o1] "+v"(tz[0][0]), [o2] "+v"(tz[1][0]), [o3] "+v"(tz[0][1]) : [i0] "v"(wf), [i1] "a"(e.tok[2][0][2]), [i2] "v"(sc.ri[0]), [i3] "v"(sc.ri[1]), [i4] "v"(sc.ri[0]));
  asm volatile("v_mfma_f32_16x16x32_bf16 %[o0], %[i0], %[i1], %[o0]\n\t" "v_fma_f32 %[o1], %[o1], %[i2], %[i2]\n\t" "v_fma_f32 %[o2], %[o2], %[i3], %[i3]\n\t" : [o0] "+v"(e.acc[3][8]), [o1] "+v"(tz[1][1]), [o2] "+v"(tz[0][2]) : [i0] "v"(wf), [i1] "a"(e.tok[2][0][3]), [i2] "v"(sc.ri[1]), [i3] "v"(sc.ri[0]));
  }
  asm volatile("s_waitcnt lgkmcnt(4)");
  { const u32x4 wf = __builtin_shufflevector(e.wlo[3], e.whi[3], 0, 1, 2, 3);
  asm volatile("v_mfma_f32_16x16x32_bf16 %[o0], %[i0], %[i1], %[o0]\n\t" "ds_read_b64_tr_b16 %[o1], %[i2] offset:0\n\t" "v_fma_f32 %[o2], %[o2], %[i3], %[i3]\n\t" : [o0] "+v"(e.acc[0][9]), [o1] "=&v"(e.wlo[2]), [o2] "+v"(tz[1][2]) : [i0] "v"(wf), [i1] "a"(e.tok[2][0][0]), [i2] "v"(e.laddr[1][4]), [i3] "v"(sc.ri[1]));
  asm volatile("v_mfma_f32_16x16x32_bf16 %[o0], %[i0], %[i1], %[o0]\n\t" "ds_read_b64_tr_b16 %[o1], %[i2] offset:4096\n\t" "v_fma_f32 %[o2], %[o2], %[i3], %[i3]\n\t" : [o0] "+v"(e.acc[1][9]), [o1] "=&v"(e.whi[2]), [o2] "+v"(tz[0][3]) : [i0] "v"(wf), [i1] "a"(e.tok[2][0][1]), [i2] "v"(e.laddr[1][4]), [i3] "v"(sc.ri[0]));
  asm volatile("v_mfma_f32_16x16x32_bf16 %[o0], %[i0], %[i1], %[o0]\n\t" "v_fma_f32 %[o1], %[o1], %[i2], %[i2]\n\t" "v_rcp_f32 %[o2], %[o2]\n\t" "v_rcp_f32 %[o3], %[o3]\n\t" : [o0] "+v"(e.acc[2][9]), [o1] "+v"(tz[1][3]), [o2] "+v"(tz[0][0]), [o3] "+v"(tz[1][0]) : [i0] "v"(wf), [i1] "a"(e.tok[2][0][2]), [i2] "v"(sc.ri[1]));
  asm volatile("v_mfma_f32_16x16x32_bf16 %[o0], %[i0], %[i1], %[o0]\n\t" "v_rcp_f32 %[o1], %[o1]\n\t" "v_rcp_f32 %[o2], %[o2]\n\t" : [o0] "+v"(e.acc[3][9]), [o1] "+v"(tz[0][1]), [o2] "+v"(tz[1][1]) : [i0] "v"(wf), [i1] "a"(e.tok[2][0][3]));
  }
  asm volatile("s_waitcnt lgkmcnt(4)");
  { const u32x4 wf = __builtin_shufflevector(e.wlo[0], e.whi[0], 0, 1, 2, 3);
  asm volatile("v_mfma_f32_16x16x32_bf16 %[o0], %[i0], %[i1], %[o0]\n\t" "ds_read_b64_tr_b16 %[o1], %[i2] offset:8192\n\t" "v_rcp_f32 %[o2], %[o2]\n\t" : [o0] "+v"(e.acc[0][8]), [o1] "=&v"(e.wlo[3]), [o2] "+v"(tz[0][2]) : [i0] "v"(wf), [i1] "a"(e.tok[3][0][0]), [i2] "v"(e.laddr[1][4]));
  asm volatile("v_mfma_f32_16x16x32_bf16 %[o0], %[i0], %[i1], %[o0]\n\t" "ds_read_b64_tr_b16 %[o1], %[i2] offset:12288\n\t" "v_rcp_f32 %[o2], %[o2]\n\t" : [o0] "+v"(e.acc[1][8]), [o1] "=&v"(e.whi[3]), [o2] "+v"(tz[1][2]) : [i0] "v"(wf), [i1] "a"(e.tok[3][0][1]), [i2] "v"(e.laddr[1][4]));
  asm volatile("v_mfma_f32_16x16x32_bf16 %[o0], %[i0], %[i1], %[o0]\n\t" "v_rcp_f32 %[o1], %[o1]\n\t" "v_rcp_f32 %[o2], %[o2]\n\t" "v_mul_f32 %[o3], %[i2], %[i3]\n\t" : [o0] "+v"(e.acc[2][8]), [o1] "+v"(tz[0][3]), [o2] "+v"(tz[1][3]), [o3] "=&v"(tw[0][0]) : [i0] "v"(wf), [i1] "a"(e.tok[3][0][2]), [i2] "v"(e.acc[0][7][0]), [i3] "v"(tz[0][0]));
  asm volatile("v_mfma_f32_16x16x32_bf16 %[o0], %[i0], %[i1], %[o0]\n\t" "v_mul_f32 %[o1], %[i2], %[i3]\n\t" "v_mul_f32 %[o2], %[i4], %[i5]\n\t" : [o0] "+v"(e.acc[3][8]), [o1] "=&v"(tw[1][0]), [o2] "=&v"(tw[0][1]) : [i0] "v"(wf), [i1] "a"(e.tok[3][0][3]), [i2] "v"(e.acc[1][7][0]), [i3] "v"(tz[1][0]), [i4] "v"(e.acc[0][7][1]), [i5] "v"(tz[0][1]));
  }
  asm volatile("s_waitcnt lgkmcnt(4)");
  { const u32x4 wf = __builtin_shufflevector(e.wlo[1], e.whi[1], 0, 1, 2, 3);
  asm volatile("v_mfma_f32_16x16x32_bf16 %[o0], %[i0], %[i1], %[o0]\n\t" "ds_read_b64_tr_b16 %[o1], %[i2] offset:16384\n\t" "v_mul_f32 %[o2], %[i3], %[i4]\n\t" : [o0] "+v"(e.acc[0][9]), [o1] "=&v"(e.wlo[0]), [o2] "=&v"(tw[1][1]) : [i0] "v"(wf), [i1] "a"(e.tok[3][0][0]), [i2] "v"(e.laddr[1][4]), [i3] "v"(e.acc[1][7][1]), [i4] "v"(tz[1][1]));
  asm volatile("v_mfma_f32_16x16x32_bf16 %[o0], %[i0], %[i1], %[o0]\n\t" "ds_read_b64_tr_b16 %[o1], %[i2] offset:20480\n\t" "v_mul_f32 %[o2], %[i3], %[i4]\n\t" : [o0] "+v"(e.acc[1][9]), [o1] "=&v"(e.whi[0]), [o2] "=&v"(tw[0][2]) : [i0] "v"(wf), [i1] "a"(e.tok[3][0][1]), [i2] "v"(e.laddr[1][4]), [i3] "v"(e.acc[0][7][2]), [i4] "v"(tz[0][2]));
  asm volatile("v_mfma_f32_16x16x32_bf16 %[o0], %[i0], %[i1], %[o0]\n\t" "v_mul_f32 %[o1], %[i2], %[i3]\n\t" "v_mul_f32 %[o2], %[i4], %[i5]\n\t" "v_mul_f32 %[o3], %[i6], %[i7]\n\t" : [o0] "+v"(e.acc[2][9]), [o1] "=&v"(tw[1][2]), [o2] "=&v"(tw[0][3]), [o3] "=&v"(tw[1][3]) : [i0] "v"(wf), [i1] "a"(e.tok[3][0][2]), [i2] "v"(e.acc[1][7][2]), [i3] "v"(tz[1][2]), [i4] "v"(e.acc[0][7][3]), [i5] "v"(tz[0][3]), [i6] "v"(e.acc[1][7][3]), [i7] "v"(tz[1][3]));
  asm volatile("v_mfma_f32_16x16x32_bf16 %[o0], %[i0], %[i1], %[o0]\n\t" "v_cvt_pk_bf16_f32 %[o1], %[i2], %[i3]\n\t" "v_cvt_pk_bf16_f32 %[o2], %[i4], %[i5]\n\t" : [o0] "+v"(e.acc[3][9]), [o1] "=&v"(pk[1][0][0]), [o2] "=&v"(pk[1][1][0]) : [i0] "v"(wf), [i1] "a"(e.tok[3][0][3]), [i2] "v"(tw[0][0]), [i3] "v"(tw[0][1]), [i4] "v"(tw[1][0]), [i5] "v"(tw[1][1]));
  }
  asm volatile("s_waitcnt lgkmcnt(4)");
  { const u32x4 wf = __builtin_shufflevector(e.wlo[2], e.whi[2], 0, 1, 2, 3);
  asm volatile("v_mfma_f32_16x16x32_bf16 %[o0], %[i0], %[i1], %[o0]\n\t" "ds_read_b64_tr_b16 %[o1], %[i2] offset:24576\n\t" "v_cvt_pk_bf16_f32 %[o2], %[i3], %[i4]\n\t" : [o0] "+v"(e.acc[0][8]), [o1] "=&v"(e.wlo[1]), [o2] "=&v"(pk[1][0][1]) : [i0] "v"(wf), [i1] "a"(e.tok[4][0][0]), [i2] "v"(e.laddr[1][4]), [i3] "v"(tw[0][2]), [i4] "v"(tw[0][3]));
  asm volatile("v_mfma_f32_16x16x32_bf16 %[o0], %[i0], %[i1], %[o0]\n\t" "ds_read_b64_tr_b16 %[o1], %[i2] offset:28672\n\t" "v_cvt_pk_bf16_f32 %[o2], %[i3], %[i4]\n\t" : [o0] "+v"(e.acc[1][8]), [o1] "=&v"(e.whi[1]), [o2] "=&v"(pk[1][1][1]) : [i0] "v"(wf), [i1] "a"(e.tok[4][0][1]), [i2] "v"(e.laddr[1][4]), [i3] "v"(tw[1][2]), [i4] "v"(tw[1][3]));
  asm volatile("v_mfma_f32_16x16x32_bf16 %[o0], %[i0], %[i1], %[o0]\n\t" : [o0] "+v"(e.acc[2][8]) : [i0] "v"(wf), [i1] "a"(e.tok[4][0][2]));
  st128a(c.wsr, p0[0] + 0, so.pair[3], (u32x4){pk[0][0][0], pk[0][0][1], pk[1][0][0], pk[1][0][1]});
  st128a(c.wsr, p0[0] + 1024, so.pair[3], (u32x4){pk[0][1][0], pk[0][1][1], pk[1][1][0], pk[1][1][1]});
  asm volatile("v_mul_f32 %[o0], %[i0], %[i1]\n\t" : [o0] "=&v"(tz[0][0]) : [i0] "v"(e.acc[2][6][0]), [i1] "v"(sc.zr[2]));
  asm volatile("v_mfma_f32_16x16x32_bf16 %[o0], %[i0], %[i1], %[o0]\n\t" "v_mul_f32 %[o1], %[i2], %[i3]\n\t" "v_mul_f32 %[o2], %[i4], %[i5]\n\t" : [o0] "+v"(e.acc[3][8]), [o1] "=&v"(tz[1][0]), [o2] "=&v"(tz[0][1]) : [i0] "v"(wf), [i1] "a"(e.tok[4][0][3]), [i2] "v"(e.acc[3][6][0]), [i3] "v"(sc.zr[3]), [i4] "v"(e.acc[2][6][1]), [i5] "v"(sc.zr[2]));
  }
  asm volatile("s_waitcnt lgkmcnt(4)");
  { const u32x4 wf = __builtin_shufflevector(e.wlo[3], e.whi[3], 0, 1, 2, 3);
  asm volatile("v_mfma_f32_16x16x32_bf16 %[o0], %[i0], %[i1], %[o0]\n\t" "ds_read_b64_tr_b16 %[o1], %[i2] offset:32768\n\t" "v_mul_f32 %[o2], %[i3], %[i4]\n\t" : [o0] "+v"(e.acc[0][9]), [o1] "=&v"(e.wlo[2]), [o2] "=&v"(tz[1][1]) : [i0] "v"(wf), [i1] "a"(e.tok[4][0][0]), [i2] "v"(e.laddr[1][4]), [i3] "v"(e.acc[3][6][1]), [i4] "v"(sc.zr[3]));
  asm volatile("v_mfma_f32_16x16x32_bf16 %[o0], %[i0], %[i1], %[o0]\n\t" "ds_read_b64_tr_b16 %[o1], %[i2] offset:36864\n\t" "v_mul_f32 %[o2], %[i3], %[i4]\n\t" : [o0] "+v"(e.acc[1][9]), [o1] "=&v"(e.whi[2]), [o2] "=&v"(tz[0][2]) : [i0] "v"(wf), [i1] "a"(e.tok[4][0][1]), [i2] "v"(e.laddr[1][4]), [i3] "v"(e.acc[2][6][2]), [i4] "v"(sc.zr[2]));
  asm volatile("v_mfma_f32_16x16x32_bf16 %[o0], %[i0], %[i1], %[o0]\n\t" "v_mul_f32 %[o1], %[i2], %[i3]\n\t" "v_mul_f32 %[o2], %[i4], %[i5]\n\t" "v_mul_f32 %[o3], %[i6], %[i7]\n\t" : [o0] "+v"(e.acc[2][9]), [o1] "=&v"(tz[1][2]), [o2] "=&v"(tz[0][3]), [o3] "=&v"(tz[1][3]) : [i0] "v"(wf), [i1] "a"(e.tok[4][0][2]), [i2] "v"(e.acc[3][6][2]), [i3] "v"(sc.zr[3]), [i4] "v"(e.acc[2][6][3]), [i5] "v"(sc.zr[2]), [i6] "v"(e.acc[3][6][3]), [i7] "v"(sc.zr[3]));
  asm volatile("v_mfma_f32_16x16x32_bf16 %[o0], %[i0], %[i1], %[o0]\n\t" "v_exp_f32 %[o1], %[o1]\n\t" "v_exp_f32 %[o2], %[o2]\n\t" : [o0] "+v"(e.acc[3][9]), [o1] "+v"(tz[0][0]), [o2] "+v"(tz[1][0]) : [i0] "v"(wf), [i1] "a"(e.tok[4][0][3]));
  }
  asm volatile("s_waitcnt lgkmcnt(4)");
  { const u32x4 wf = __builtin_shufflevector(e.wlo[0], e.whi[0], 0, 1, 2, 3);
  asm volatile("v_mfma_f32_16x16x32_bf16 %[o0], %[i0], %[i1], %[o0]\n\t" "ds_read_b64_tr_b16 %[o1], %[i2] offset:40960\n\t" "v_exp_f32 %[o2], %[o2]\n\t" : [o0] "+v"(e.acc[0][8]), [o1] "=&v"(e.wlo[3]), [o2] "+v"(tz[0][1]) : [i0] "v"(wf), [i1] "a"(e.tok[5][0][0]), [i2] "v"(e.laddr[1][4]));
  asm volatile("v_mfma_f32_16x16x32_bf16 %[o0], %[i0], %[i1], %[o0]\n\t" "ds_read_b64_tr_b16 %[o1], %[i2] offset:45056\n\t" "v_exp_f32 %[o2], %[o2]\n\t" : [o0] "+v"(e.acc[1][8]), [o1] "=&v"(e.whi[3]), [o2] "+v"(tz[1][1]) : [i0] "v"(wf), [i1] "a"(e.tok[5][0][1]), [i2] "v"(e.laddr[1][4]));
  asm volatile("v_mfma_f32_16x16x32_bf16 %[o0], %[i0], %[i1], %[o0]\n\t" "v_exp_f32 %[o1], %[o1]\n\t" "v_exp_f32 %[o2], %[o2]\n\t" "v_exp_f32 %[o3], %[o3]\n\t" : [o0] "+v"(e.acc[2][8]), [o1] "+v"(tz[0][2]), [o2] "+v"(tz[1][2]), [o3] "+v"(tz[0][3]) : [i0] "v"(wf), [i1] "a"(e.tok[5][0][2]));
  asm volatile("v_mfma_f32_16x16x32_bf16 %[o0], %[i0], %[i1], %[o0]\n\t" "v_exp_f32 %[o1], %[o1]\n\t" "v_fma_f32 %[o2], %[o2], %[i2], %[i2]\n\t" : [o0] "+v"(e.acc[3][8]), [o1] "+v"(tz[1][3]), [o2] "+v"(tz[0][0]) : [i0] "v"(wf), [i1] "a"(e.tok[5][0][3]), [i2] "v"(sc.ri[2]));
  }
  asm volatile("s_waitcnt lgkmcnt(4)");
  { const u32x4 wf = __builtin_shufflevector(e.wlo[1], e.whi[1], 0, 1, 2, 3);
  asm volatile("v_mfma_f32_16x16x32_bf16 %[o0], %[i0], %[i1], %[o0]\n\t" "ds_read_b64_tr_b16 %[o1], %[i2] offset:49152\n\t" "v_fma_f32 %[o2], %[o2], %[i3], %[i3]\n\t" : [o0] "+v"(e.acc[0][9]), [o1] "=&v"(e.wlo[0]), [o2] "+v"(tz[1][0]) : [i0] "v"(wf), [i1] "a"(e.tok[5][0][0]), [i2] "v"(e.laddr[1][4]), [i3] "v"(sc.ri[3]));
  asm volatile("v_mfma_f32_16x16x32_bf16 %[o0], %[i0], %[i1], %[o0]\n\t" "ds_read_b64_tr_b16 %[o1], %[i2] offset:53248\n\t" "v_fma_f32 %[o2], %[o2], %[i3], %[i3]\n\t" : [o0] "+v"(e.acc[1][9]), [o1] "=&v"(e.whi[0]), [o2] "+v"(tz[0][1]) : [i0] "v"(wf), [i1] "a"(e.tok[5][0][1]), [i2] "v"(e.laddr[1][4]), [i3] "v"(sc.ri[2]));
  asm volatile("v_mfma_f32_16x16x32_bf16 %[o0], %[i0], %[i1], %[o0]\n\t" "v_fma_f32 %[o1], %[o1], %[i2], %[i2]\n\t" "v_fma_f32 %[o2], %[o2], %[i3], %[i3]\n\t" "v_fma_f32 %[o3], %[o3], %[i4], %[i4]\n\t" : [o0] "+v"(e.acc[2][9]), [o1] "+v"(tz[1][1]), [o2] "+v"(tz[0][2]), [o3] "+v"(tz[1][2]) : [i0] "v"(wf), [i1] "a"(e.tok[5][0][2]), [i2] "v"(sc.ri[3]), [i3] "v"(sc.ri[2]), [i4] "v"(sc.ri[3]));
  asm volatile("v_mfma_f32_16x16x32_bf16 %[o0], %[i0], %[i1], %[o0]\n\t" "v_fma_f32 %[o1], %[o1], %[i2], %[i2]\n\t" "v_fma_f32 %[o2], %[o2], %[i3], %[i3]\n\t" : [o0] "+v"(e.acc[3][9]), [o1] "+v"(tz[0][3]), [o2] "+v"(tz[1][3]) : [i0] "v"(wf), [i1] "a"(e.tok[5][0][3]), [i2] "v"(sc.ri[2]), [i3] "v"(sc.ri[3]));
  }
  asm volatile("s_waitcnt lgkmcnt(4)");
  { const u32x4 wf = __builtin_shufflevector(e.wlo[2], e.whi[2], 0, 1, 2, 3);
  asm volatile("v_mfma_f32_16x16x32_bf16 %[o0], %[i0], %[i1], %[o0]\n\t" "ds_read_b64_tr_b16 %[o1], %[i2] offset:57344\n\t" "v_rcp_f32 %[o2], %[o2]\n\t" : [o0] "+v"(e.acc[0][8]), [o1] "=&v"(e.wlo[1]), [o2] "+v"(tz[0][0]) : [i0] "v"(wf), [i1] "a"(e.tok[6][0][0]), [i2] "v"(e.laddr[1][4]));
  asm volatile("v_mfma_f32_16x16x32_bf16 %[o0], %[i0], %[i1], %[o0]\n\t" "ds_read_b64_tr_b16 %[o1], %[i2] offset:61440\n\t" "v_rcp_f32 %[o2], %[o2]\n\t" : [o0] "+v"(e.acc[1][8]), [o1] "=&v"(e.whi[1]), [o2] "+v"(tz[1][0]) : [i0] "v"(wf), [i1] "a"(e.tok[6][0][1]), [i2] "v"(e.laddr[1][4]));
  asm volatile("v_mfma_f32_16x16x32_bf16 %[o0], %[i0], %[i1], %[o0]\n\t" "v_rcp_f32 %[o1], %[o1]\n\t" "v_rcp_f32 %[o2], %[o2]\n\t" "v_rcp_f32 %[o3], %[o3]\n\t" : [o0] "+v"(e.acc[2][8]), [o1] "+v"(tz[0][1]), [o2] "+v"(tz[1][1]), [o3] "+v"(tz[0][2]) : [i0] "v"(wf), [i1] "a"(e.tok[6][0][2]));
  asm volatile("v_mfma_f32_16x16x32_bf16 %[o0], %[i0], %[i1], %[o0]\n\t" "v_rcp_f32 %[o1], %[o1]\n\t" "v_rcp_f32 %[o2], %[o2]\n\t" : [o0] "+v"(e.acc[3][8]), [o1] "+v"(tz[1][2]), [o2] "+v"(tz[0][3]) : [i0] "v"(wf), [i1] "a"(e.tok[6][0][3]));
  }
  asm volatile("s_waitcnt lgkmcnt(4)");
  { const u32x4 wf = __builtin_shufflevector(e.wlo[3], e.whi[3], 0, 1, 2, 3);
  asm volatile("v_mfma_f32_16x16x32_bf16 %[o0], %[i0], %[i1], %[o0]\n\t" "ds_read_b64_tr_b16 %[o1], %[i2] offset:16384\n\t" "v_rcp_f32 %[o2], %[o2]\n\t" : [o0] "+v"(e.acc[0][9]), [o1] "=&v"(e.wlo[2]), [o2] "+v"(tz[1][3]) : [i0] "v"(wf), [i1] "a"(e.tok[6][0][0]), [i2] "v"(e.laddr[0][5]));
  asm volatile("v_mfma_f32_16x16x32_bf16 %[o0], %[i0], %[i1], %[o0]\n\t" "ds_read_b64_tr_b16 %[o1], %[i2] offset:20480\n\t" "v_mul_f32 %[o2], %[i3], %[i4]\n\t" : [o0] "+v"(e.acc[1][9]), [o1] "=&v"(e.whi[2]), [o2] "=&v"(tw[0][0]) : [i0] "v"(wf), [i1] "a"(e.tok[6][0][1]), [i2] "v"(e.laddr[0][5]), [i3] "v"(e.acc[2][7][0]), [i4] "v"(tz[0][0]));
  asm volatile("v_mfma_f32_16x16x32_bf16 %[o0], %[i0], %[i1], %[o0]\n\t" "v_mul_f32 %[o1], %[i2], %[i3]\n\t" "v_mul_f32 %[o2], %[i4], %[i5]\n\t" "v_mul_f32 %[o3], %[i6], %[i7]\n\t" : [o0] "+v"(e.acc[2][9]), [o1] "=&v"(tw[1][0]), [o2] "=&v"(tw[0][1]), [o3] "=&v"(tw[1][1]) : [i0] "v"(wf), [i1] "a"(e.tok[6][0][2]), [i2] "v"(e.acc[3][7][0]), [i3] "v"(tz[1][0]), [i4] "v"(e.acc[2][7][1]), [i5] "v"(tz[0][1]), [i6] "v"(e.acc[3][7][1]), [i7] "v"(tz[1][1]));
  asm volatile("v_mfma_f32_16x16x32_bf16 %[o0], %[i0], %[i1], %[o0]\n\t" "v_mul_f32 %[o1], %[i2], %[i3]\n\t" "v_mul_f32 %[o2], %[i4], %[i5]\n\t" : [o0] "+v"(e.acc[3][9]), [o1] "=&v"(tw[0][2]), [o2] "=&v"(tw[1][2]) : [i0] "v"(wf), [i1] "a"(e.tok[6][0][3]), [i2] "v"(e.acc[2][7][2]), [i3] "v"(tz[0][2]), [i4] "v"(e.acc[3][7][2]), [i5] "v"(tz[1][2]));
  }
  asm volatile("s_waitcnt lgkmcnt(4)");
  { const u32x4 wf = __builtin_shufflevector(e.wlo[0], e.whi[0], 0, 1, 2, 3);
  asm volatile("v_mfma_f32_16x16x32_bf16 %[o0], %[i0], %[i1], %[o0]\n\t" "ds_read_b64_tr_b16 %[o1], %[i2] offset:24576\n\t" "v_mul_f32 %[o2], %[i3], %[i4]\n\t" : [o0] "+v"(e.acc[0][8]), [o1] "=&v"(e.wlo[3]), [o2] "=&v"(tw[0][3]) : [i0] "v"(wf), [i1] "a"(e.tok[7][0][0]), [i2] "v"(e.laddr[0][5]), [i3] "v"(e.acc[2][7][3]), [i4] "v"(tz[0][3]));
  asm volatile("v_mfma_f32_16x16x32_bf16 %[o0], %[i0], %[i1], %[o0]\n\t" "ds_read_b64_tr_b16 %[o1], %[i2] offset:28672\n\t" "v_mul_f32 %[o2], %[i3], %[i4]\n\t" : [o0] "+v"(e.acc[1][8]), [o1] "=&v"(e.whi[3]), [o2] "=&v"(tw[1][3]) : [i0] "v"(wf), [i1] "a"(e.tok[7][0][1]), [i2] "v"(e.laddr[0][5]), [i3] "v"(e.acc[3][7][3]), [i4] "v"(tz[1][3]));
  asm volatile("v_mfma_f32_16x16x32_bf16 %[o0], %[i0], %[i1], %[o0]\n\t" "v_cvt_pk_bf16_f32 %[o1], %[i2], %[i3]\n\t" "v_cvt_pk_bf16_f32 %[o2], %[i4], %[i5]\n\t" "v_cvt_pk_bf16_f32 %[o3], %[i6], %[i7]\n\t" : [o0] "+v"(e.acc[2][8]), [o1] "=&v"(pk[1][2][0]), [o2] "=&v"(pk[1][3][0]), [o3] "=&v"(pk[1][2][1]) : [i0] "v"(wf), [i1] "a"(e.tok[7][0][2]), [i2] "v"(tw[0][0]), [i3] "v"(tw[0][1]), [i4] "v"(tw[1][0]), [i5] "v"(tw[1][1]), [i6] "v"(tw[0][2]), [i7] "v"(tw[0][3]));
  asm volatile("v_mfma_f32_16x16x32_bf16 %[o0], %[i0], %[i1], %[o0]\n\t" "v_cvt_pk_bf16_f32 %[o1], %[i2], %[i3]\n\t" : [o0] "+v"(e.acc[3][8]), [o1] "=&v"(pk[1][3][1]) : [i0] "v"(wf), [i1] "a"(e.tok[7][0][3]), [i2] "v"(tw[1][2]), [i3] "v"(tw[1][3]));
  st128a(c.wsr, p0[1] + 0, so.pair[3], (u32x4){pk[0][2][0], pk[0][2][1], pk[1][2][0], pk[1][2][1]});
  }
  asm volatile("s_waitcnt lgkmcnt(4)");
  { const u32x4 wf = __builtin_shufflevector(e.wlo[1], e.whi[1], 0, 1, 2, 3);
  asm volatile("v_mfma_f32_16x16x32_bf16 %[o0], %[i0], %[i1], %[o0]\n\t" "ds_read_b64_tr_b16 %[o1], %[i2] offset:32768\n\t" : [o0] "+v"(e.acc[0][9]), [o1] "=&v"(e.wlo[0]) : [i0] "v"(wf), [i1] "a"(e.tok[7][0][0]), [i2] "v"(e.laddr[0][5]));
  st128a(c.wsr, p0[1] + 1024, so.pair[3], (u32x4){pk[0][3][0], pk[0][3][1], pk[1][3][0], pk[1][3][1]});
  asm volatile("v_mfma_f32_16x16x32_bf16 %[o0], %[i0], %[i1], %[o0]\n\t" "ds_read_b64_tr_b16 %[o1], %[i2] offset:36864\n\t" : [o0] "+v"(e.acc[1][9]), [o1] "=&v"(e.whi[0]) : [i0] "v"(wf), [i1] "a"(e.tok[7][0][1]), [i2] "v"(e.laddr[0][5]));
  asm volatile("v_mfma_f32_16x16x32_bf16 %[o0], %[i0], %[i1], %[o0]\n\t" : [o0] "+v"(e.acc[2][9]) : [i0] "v"(wf), [i1] "a"(e.tok[7][0][2]));
  asm volatile("v_mfma_f32_16x16x32_bf16 %[o0], %[i0], %[i1], %[o0]\n\t" : [o0] "+v"(e.acc[3][9]) : [i0] "v"(wf), [i1] "a"(e.tok[7][0][3]));
  }
  asm volatile("s_waitcnt lgkmcnt(4)");
  { const u32x4 wf = __builtin_shufflevector(e.wlo[2], e.whi[2], 0, 1, 2, 3);
  asm volatile("v_mfma_f32_16x16x32_bf16 %[o0], %[i0], %[i1], %[o0]\n\t" "ds_read_b64_tr_b16 %[o1], %[i2] offset:40960\n\t" "v_mul_f32 %[o2], %[i3], %[i4]\n\t" : [o0] "+a"(e.acc[0][10]), [o1] "=&v"(e.wlo[1]), [o2] "=&v"(tz[0][0]) : [i0] "v"(wf), [i1] "a"(e.tok[1][0][0]), [i2] "v"(e.laddr[0][5]), [i3] "v"(e.acc[0][8][0]), [i4] "v"(sc.zr[0]));
  asm volatile("v_mfma_f32_16x16x32_bf16 %[o0], %[i0], %[i1], %[o0]\n\t" "ds_read_b64_tr_b16 %[o1], %[i2] offset:45056\n\t" "v_mul_f32 %[o2], %[i3], %[i4]\n\t" : [o0] "+a"(e.acc[1][10]), [o1] "=&v"(e.whi[1]), [o2] "=&v"(tz[1][0]) : [i0] "v"(wf), [i1] "a"(e.tok[1][0][1]), [i2] "v"(e.laddr[0][5]), [i3] "v"(e.acc[1][8][0]), [i4] "v"(sc.zr[1]));
  asm volatile("v_mfma_f32_16x16x32_bf16 %[o0], %[i0], %[i1], %[o0]\n\t" "v_mul_f32 %[o1], %[i2], %[i3]\n\t" "v_mul_f32 %[o2], %[i4], %[i5]\n\t" "v_mul_f32 %[o3], %[i6], %[i7]\n\t" : [o0] "+a"(e.acc[2][10]), [o1] "=&v"(tz[0][1]), [o2] "=&v"(tz[1][1]), [o3] "=&v"(tz[0][2]) : [i0] "v"(wf), [i1] "a"(e.tok[1][0][2]), [i2] "v"(e.acc[0][8][1]), [i3] "v"(sc.zr[0]), [i4] "v"(e.acc[1][8][1]), [i5] "v"(sc.zr[1]), [i6] "v"(e.acc[0][8][2]), [i7] "v"(sc.zr[0]));
  asm volatile("v_mfma_f32_16x16x32_bf16 %[o0], %[i0], %[i1], %[o0]\n\t" "v_mul_f32 %[o1], %[i2], %[i3]\n\t" "v_mul_f32 %[o2], %[i4], %[i5]\n\t" : [o0] "+a"(e.acc[3][10]), [o1] "=&v"(tz[1][2]), [o2] "=&v"(tz[0][3]) : [i0] "v"(wf), [i1] "a"(e.tok[1][0][3]), [i2] "v"(e.acc[1][8][2]), [i3] "v"(sc.zr[1]), [i4] "v"(e.acc[0][8][3]), [i5] "v"(sc.zr[0]));
  }
  asm volatile("s_waitcnt lgkmcnt(4)");
  { const u32x4 wf = __builtin_shufflevector(e.wlo[3], e.whi[3], 0, 1, 2, 3);
  asm volatile("v_mfma_f32_16x16x32_bf16 %[o0], %[i0], %[i1], %[o0]\n\t" "ds_read_b64_tr_b16 %[o1], %[i2] offset:49152\n\t" "v_mul_f32 %[o2], %[i3], %[i4]\n\t" : [o0] "+a"(e.acc[0][11]), [o1] "=&v"(e.wlo[2]), [o2] "=&v"(tz[1][3]) : [i0] "v"(wf), [i1] "a"(e.tok[1][0][0]), [i2] "v"(e.laddr[0][5]), [i3] "v"(e.acc[1][8][3]), [i4] "v"(sc.zr[1]));
  asm volatile("v_mfma_f32_16x16x32_bf16 %[o0], %[i0], %[i1], %[o0]\n\t" "ds_read_b64_tr_b16 %[o1], %[i2] offset:53248\n\t" "v_exp_f32 %[o2], %[o2]\n\t" : [o0] "+a"(e.acc[1][11]), [o1] "=&v"(e.whi[2]), [o2] "+v"(tz[0][0]) : [i0] "v"(wf), [i1] "a"(e.tok[1][0][1]), [i2] "v"(e.laddr[0][5]));
  asm volatile("v_mfma_f32_16x16x32_bf16 %[o0], %[i0], %[i1], %[o0]\n\t" "v_exp_f32 %[o1], %[o1]\n\t" "v_exp_f32 %[o2], %[o2]\n\t" "v_exp_f32 %[o3], %[o3]\n\t" : [o0] "+a"(e.acc[2][11]), [o1] "+v"(tz[1][0]), [o2] "+v"(tz[0][1]), [o3] "+v"(tz[1][1]) : [i0] "v"(wf), [i1] "a"(e.tok[1][0][2]));
  asm volatile("v_mfma_f32_16x16x32_bf16 %[o0], %[i0], %[i1], %[o0]\n\t" "v_exp_f32 %[o1], %[o1]\n\t" "v_exp_f32 %[o2], %[o2]\n\t" : [o0] "+a"(e.acc[3][11]), [o1] "+v"(tz[0][2]), [o2] "+v"(tz[1][2]) : [i0] "v"(wf), [i1] "a"(e.tok[1][0][3]));
  }
  asm volatile("s_waitcnt lgkmcnt(4)");
  { const u32x4 wf = __builtin_shufflevector(e.wlo[0], e.whi[0], 0, 1, 2, 3);
  asm volatile("v_mfma_f32_16x16x32_bf16 %[o0], %[i0], %[i1], %[o0]\n\t" "ds_read_b64_tr_b16 %[o1], %[i2] offset:57344\n\t" "v_exp_f32 %[o2], %[o2]\n\t" : [o0] "+a"(e.acc[0][10]), [o1] "=&v"(e.wlo[3]), [o2] "+v"(tz[0][3]) : [i0] "v"(wf), [i1] "a"(e.tok[2][0][0]), [i2] "v"(e.laddr[0][5]));
  asm volatile("v_mfma_f32_16x16x32_bf16 %[o0], %[i0], %[i1], %[o0]\n\t" "ds_read_b64_tr_b16 %[o1], %[i2] offset:61440\n\t" "v_exp_f32 %[o2], %[o2]\n\t" : [o0] "+a"(e.acc[1][10]), [o1] "=&v"(e.whi[3]), [o2] "+v"(tz[1][3]) : [i0] "v"(wf), [i1] "a"(e.tok[2][0][1]), [i2] "v"(e.laddr[0][5]));
  asm volatile("v_mfma_f32_16x16x32_bf16 %[o0], %[i0], %[i1], %[o0]\n\t" "v_fma_f32 %[o1], %[o1], %[i2], %[i2]\n\t" "v_fma_f32 %[o2], %[o2], %[i3], %[i3]\n\t" "v_fma_f32 %[o3], %[o3], %[i4], %[i4]\n\t" : [o0] "+a"(e.acc[2][10]), [o1] "+v"(tz[0][0]), [o2] "+v"(tz[1][0]), [o3] "+v"(tz[0][1]) : [i0] "v"(wf), [i1] "a"(e.tok[2][0][2]), [i2] "v"(sc.ri[0]), [i3] "v"(sc.ri[1]), [i4] "v"(sc.ri[0]));
  asm volatile("v_mfma_f32_16x16x32_bf16 %[o0], %[i0], %[i1], %[o0]\n\t" "v_fma_f32 %[o1], %[o1], %[i2], %[i2]\n\t" "v_fma_f32 %[o2], %[o2], %[i3], %[i3]\n\t" : [o0] "+a"(e.acc[3][10]), [o1] "+v"(tz[1][1]), [o2] "+v"(tz[0][2]) : [i0] "v"(wf), [i1] "a"(e.tok[2][0][3]), [i2] "v"(sc.ri[1]), [i3] "v"(sc.ri[0]));
  }
  asm volatile("s_waitcnt lgkmcnt(4)");
  { const u32x4 wf = __builtin_shufflevector(e.wlo[1], e.whi[1], 0, 1, 2, 3);
  asm volatile("v_mfma_f32_16x16x32_bf16 %[o0], %[i0], %[i1], %[o0]\n\t" "ds_read_b64_tr_b16 %[o1], %[i2] offset:0\n\t" "v_fma_f32 %[o2], %[o2], %[i3], %[i3]\n\t" : [o0] "+a"(e.acc[0][11]), [o1] "=&v"(e.wlo[0]), [o2] "+v"(tz[1][2]) : [i0] "v"(wf), [i1] "a"(e.tok[2][0][0]), [i2] "v"(e.laddr[1][5]), [i3] "v"(sc.ri[1]));
  asm volatile("v_mfma_f32_16x16x32_bf16 %[o0], %[i0], %[i1], %[o0]\n\t" "ds_read_b64_tr_b16 %[o1], %[i2] offset:4096\n\t" "v_fma_f32 %[o2], %[o2], %[i3], %[i3]\n\t" : [o0] "+a"(e.acc[1][11]), [o1] "=&v"(e.whi[0]), [o2] "+v"(tz[0][3]) : [i0] "v"(wf), [i1] "a"(e.tok[2][0][1]), [i2] "v"(e.laddr[1][5]), [i3] "v"(sc.ri[0]));
  asm volatile("v_mfma_f32_16x16x32_bf16 %[o0], %[i0], %[i1], %[o0]\n\t" "v_fma_f32 %[o1], %[o1], %[i2], %[i2]\n\t" "v_rcp_f32 %[o2], %[o2]\n\t" "v_rcp_f32 %[o3], %[o3]\n\t" : [o0] "+a"(e.acc[2][11]), [o1] "+v"(tz[1][3]), [o2] "+v"(tz[0][0]), [o3] "+v"(tz[1][0]) : [i0] "v"(wf), [i1] "a"(e.tok[2][0][2]), [i2] "v"(sc.ri[1]));
  asm volatile("v_mfma_f32_16x16x32_bf16 %[o0], %[i0], %[i1], %[o0]\n\t" "v_rcp_f32 %[o1], %[o1]\n\t" "v_rcp_f32 %[o2], %[o2]\n\t" : [o0] "+a"(e.acc[3][11]), [o1] "+v"(tz[0][1]), [o2] "+v"(tz[1][1]) : [i0] "v"(wf), [i1] "a"(e.tok[2][0][3]));
  }
  asm volatile("s_waitcnt lgkmcnt(4)");
  { const u32x4 wf = __builtin_shufflevector(e.wlo[2], e.whi[2], 0, 1, 2, 3);
  asm volatile("v_mfma_f32_16x16x32_bf16 %[o0], %[i0], %[i1], %[o0]\n\t" "ds_read_b64_tr_b16 %[o1], %[i2] offset:8192\n\t" "v_rcp_f32 %[o2], %[o2]\n\t" : [o0] "+a"(e.acc[0][10]), [o1] "=&v"(e.wlo[1]), [o2] "+v"(tz[0][2]) : [i0] "v"(wf), [i1] "a"(e.tok[3][0][0]), [i2] "v"(e.laddr[1][5]));
  asm volatile("v_mfma_f32_16x16x32_bf16 %[o0], %[i0], %[i1], %[o0]\n\t" "ds_read_b64_tr_b16 %[o1], %[i2] offset:12288\n\t" "v_rcp_f32 %[o2], %[o2]\n\t" : [o0] "+a"(e.acc[1][10]), [o1] "=&v"(e.whi[1]), [o2] "+v"(tz[1][2]) : [i0] "v"(wf), [i1] "a"(e.tok[3][0][1]), [i2] "v"(e.laddr[1][5]));
  asm volatile("v_mfma_f32_16x16x32_bf16 %[o0], %[i0], %[i1], %[o0]\n\t" "v_rcp_f32 %[o1], %[o1]\n\t" "v_rcp_f32 %[o2], %[o2]\n\t" "v_mul_f32 %[o3], %[i2], %[i3]\n\t" : [o0] "+a"(e.acc[2][10]), [o1] "+v"(tz[0][3]), [o2] "+v"(tz[1][3]), [o3] "=&v"(tw[0][0]) : [i0] "v"(wf), [i1] "a"(e.tok[3][0][2]), [i2] "v"(e.acc[0][9][0]), [i3] "v"(tz[0][0]));
  asm volatile("v_mfma_f32_16x16x32_bf16 %[o0], %[i0], %[i1], %[o0]\n\t" "v_mul_f32 %[o1], %[i2], %[i3]\n\t" "v_mul_f32 %[o2], %[i4], %[i5]\n\t" : [o0] "+a"(e.acc[3][10]), [o1] "=&v"(tw[1][0]), [o2] "=&v"(tw[0][1]) : [i0] "v"(wf), [i1] "a"(e.tok[3][0][3]), [i2] "v"(e.acc[1][9][0]), [i3] "v"(tz[1][0]), [i4] "v"(e.acc[0][9][1]), [i5] "v"(tz[0][1]));
  }
  asm volatile("s_waitcnt lgkmcnt(4)");
  { const u32x4 wf = __builtin_shufflevector(e.wlo[3], e.whi[3], 0, 1, 2, 3);
  asm volatile("v_mfma_f32_16x16x32_bf16 %[o0], %[i0], %[i1], %[o0]\n\t" "ds_read_b64_tr_b16 %[o1], %[i2] offset:16384\n\t" "v_mul_f32 %[o2], %[i3], %[i4]\n\t" : [o0] "+a"(e.acc[0][11]), [o1] "=&v"(e.wlo[2]), [o2] "=&v"(tw[1][1]) : [i0] "v"(wf), [i1] "a"(e.tok[3][0][0]), [i2] "v"(e.laddr[1][5]), [i3] "v"(e.acc[1][9][1]), [i4] "v"(tz[1][1]));
  asm volatile("v_mfma_f32_16x16x32_bf16 %[o0], %[i0], %[i1], %[o0]\n\t" "ds_read_b64_tr_b16 %[o1], %[i2] offset:20480\n\t" "v_mul_f32 %[o2], %[i3], %[i4]\n\t" : [o0] "+a"(e.acc[1][11]), [o1] "=&v"(e.whi[2]), [o2] "=&v"(tw[0][2]) : [i0] "v"(wf), [i1] "a"(e.tok[3][0][1]), [i2] "v"(e.laddr[1][5]), [i3] "v"(e.acc[0][9][2]), [i4] "v"(tz[0][2]));
  asm volatile("v_mfma_f32_16x16x32_bf16 %[o0], %[i0], %[i1], %[o0]\n\t" "v_mul_f32 %[o1], %[i2], %[i3]\n\t" "v_mul_f32 %[o2], %[i4], %[i5]\n\t" "v_mul_f32 %[o3], %[i6], %[i7]\n\t" : [o0] "+a"(e.acc[2][11]), [o1] "=&v"(tw[1][2]), [o2] "=&v"(tw[0][3]), [o3] "=&v"(tw[1][3]) : [i0] "v"(wf), [i1] "a"(e.tok[3][0][2]), [i2] "v"(e.acc[1][9][2]), [i3] "v"(tz[1][2]), [i4] "v"(e.acc[0][9][3]), [i5] "v"(tz[0][3]), [i6] "v"(e.acc[1][9][3]), [i7] "v"(tz[1][3]));
  asm volatile("v_mfma_f32_16x16x32_bf16 %[o0], %[i0], %[i1], %[o0]\n\t" "v_cvt_pk_bf16_f32 %[o1], %[i2], %[i3]\n\t" "v_cvt_pk_bf16_f32 %[o2], %[i4], %[i5]\n\t" : [o0] "+a"(e.acc[3][11]), [o1] "=&v"(pk[0][0][0]), [o2] "=&v"(pk[0][1][0]) : [i0] "v"(wf), [i1] "a"(e.tok[3][0][3]), [i2] "v"(tw[0][0]), [i3] "v"(tw[0][1]), [i4] "v"(tw[1][0]), [i5] "v"(tw[1][1]));
  }
  asm volatile("s_waitcnt lgkmcnt(4)");
  { const u32x4 wf = __builtin_shufflevector(e.wlo[0], e.whi[0], 0, 1, 2, 3);
  asm volatile("v_mfma_f32_16x16x32_bf16 %[o0], %[i0], %[i1], %[o0]\n\t" "ds_read_b64_tr_b16 %[o1], %[i2] offset:24576\n\t" "v_cvt_pk_bf16_f32 %[o2], %[i3], %[i4]\n\t" : [o0] "+a"(e.acc[0][10]), [o1] "=&v"(e.wlo[3]), [o2] "=&v"(pk[0][0][1]) : [i0] "v"(wf), [i1] "a"(e.tok[4][0][0]), [i2] "v"(e.laddr[1][5]), [i3] "v"(tw[0][2]), [i4] "v"(tw[0][3]));
  asm volatile("v_mfma_f32_16x16x32_bf16 %[o0], %[i0], %[i1], %[o0]\n\t" "ds_read_b64_tr_b16 %[o1], %[i2] offset:28672\n\t" "v_cvt_pk_bf16_f32 %[o2], %[i3], %[i4]\n\t" : [o0] "+a"(e.acc[1][10]), [o1] "=&v"(e.whi[3]), [o2] "=&v"(pk[0][1][1]) : [i0] "v"(wf), [i1] "a"(e.tok[4][0][1]), [i2] "v"(e.laddr[1][5]), [i3] "v"(tw[1][2]), [i4] "v"(tw[1][3]));
  asm volatile("v_mfma_f32_16x16x32_bf16 %[o0], %[i0], %[i1], %[o0]\n\t" : [o0] "+a"(e.acc[2][10]) : [i0] "v"(wf), [i1] "a"(e.tok[4][0][2]));
  st128a(c.wsr, p0[0] + 0, so.pair[4], (u32x4){pk[1][0][0], pk[1][0][1], pk[0][0][0], pk[0][0][1]});
  st128a(c.wsr, p0[0] + 1024, so.pair[4], (u32x4){pk[1][1][0], pk[1][1][1], pk[0][1][0], pk[0][1][1]});
  asm volatile("v_mul_f32 %[o0], %[i0], %[i1]\n\t" : [o0] "=&v"(tz[0][0]) : [i0] "v"(e.acc[2][8][0]), [i1] "v"(sc.zr[2]));
  asm volatile("v_mfma_f32_16x16x32_bf16 %[o0], %[i0], %[i1], %[o0]\n\t" "v_mul_f32 %[o1], %[i2], %[i3]\n\t" "v_mul_f32 %[o2], %[i4], %[i5]\n\t" : [o0] "+a"(e.acc[3][10]), [o1] "=&v"(tz[1][0]), [o2] "=&v"(tz[0][1]) : [i0] "v"(wf), [i1] "a"(e.tok[4][0][3]), [i2] "v"(e.acc[3][8][0]), [i3] "v"(sc.zr[3]), [i4] "v"(e.acc[2][8][1]), [i5] "v"(sc.zr[2]));
  }
  asm volatile("s_waitcnt lgkmcnt(4)");
  { const u32x4 wf = __builtin_shufflevector(e.wlo[1], e.whi[1], 0, 1, 2, 3);
  asm volatile("v_mfma_f32_16x16x32_bf16 %[o0], %[i0], %[i1], %[o0]\n\t" "ds_read_b64_tr_b16 %[o1], %[i2] offset:32768\n\t" "v_mul_f32 %[o2], %[i3], %[i4]\n\t" : [o0] "+a"(e.acc[0][11]), [o1] "=&v"(e.wlo[0]), [o2] "=&v"(tz[1][1]) : [i0] "v"(wf), [i1] "a"(e.tok[4][0][0]), [i2] "v"(e.laddr[1][5]), [i3] "v"(e.acc[3][8][1]), [i4] "v"(sc.zr[3]));
  asm volatile("v_mfma_f32_16x16x32_bf16 %[o0], %[i0], %[i1], %[o0]\n\t" "ds_read_b64_tr_b16 %[o1], %[i2] offset:36864\n\t" "v_mul_f32 %[o2], %[i3], %[i4]\n\t" : [o0] "+a"(e.acc[1][11]), [o1] "=&v"(e.whi[0]), [o2] "=&v"(tz[0][2]) : [i0] "v"(wf), [i1] "a"(e.tok[4][0][1]), [i2] "v"(e.laddr[1][5]), [i3] "v"(e.acc[2][8][2]), [i4] "v"(sc.zr[2]));
  asm volatile("v_mfma_f32_16x16x32_bf16 %[o0], %[i0], %[i1], %[o0]\n\t" "v_mul_f32 %[o1], %[i2], %[i3]\n\t" "v_mul_f32 %[o2], %[i4], %[i5]\n\t" "v_mul_f32 %[o3], %[i6], %[i7]\n\t" : [o0] "+a"(e.acc[2][11]), [o1] "=&v"(tz[1][2]), [o2] "=&v"(tz[0][3]), [o3] "=&v"(tz[1][3]) : [i0] "v"(wf), [i1] "a"(e.tok[4][0][2]), [i2] "v"(e.acc[3][8][2]), [i3] "v"(sc.zr[3]), [i4] "v"(e.acc[2][8][3]), [i5] "v"(sc.zr[2]), [i6] "v"(e.acc[3][8][3]), [i7] "v"(sc.zr[3]));
  asm volatile("v_mfma_f32_16x16x32_bf16 %[o0], %[i0], %[i1], %[o0]\n\t" "v_exp_f32 %[o1], %[o1]\n\t" "v_exp_f32 %[o2], %[o2]\n\t" : [o0] "+a"(e.acc[3][11]), [o1] "+v"(tz[0][0]), [o2] "+v"(tz[1][0]) : [i0] "v"(wf), [i1] "a"(e.tok[4][0][3]));
  }
  asm volatile("s_waitcnt lgkmcnt(4)");
  { const u32x4 wf = __builtin_shufflevector(e.wlo[2], e.whi[2], 0, 1, 2, 3);
  asm volatile("v_mfma_f32_16x16x32_bf16 %[o0], %[i0], %[i1], %[o0]\n\t" "ds_read_b64_tr_b16 %[o1], %[i2] offset:40960\n\t" "v_exp_f32 %[o2], %[o2]\n\t" : [o0] "+a"(e.acc[0][10]), [o1] "=&v"(e.wlo[1]), [o2] "+v"(tz[0][1]) : [i0] "v"(wf), [i1] "a"(e.tok[5][0][0]), [i2] "v"(e.laddr[1][5]));
  asm volatile("v_mfma_f32_16x16x32_bf16 %[o0], %[i0], %[i1], %[o0]\n\t" "ds_read_b64_tr_b16 %[o1], %[i2] offset:45056\n\t" "v_exp_f32 %[o2], %[o2]\n\t" : [o0] "+a"(e.acc[1][10]), [o1] "=&v"(e.whi[1]), [o2] "+v"(tz[1][1]) : [i0] "v"(wf), [i1] "a"(e.tok[5][0][1]), [i2] "v"(e.laddr[1][5]));
  asm volatile("v_mfma_f32_16x16x32_bf16 %[o0], %[i0], %[i1], %[o0]\n\t" "v_exp_f32 %[o1], %[o1]\n\t" "v_exp_f32 %[o2], %[o2]\n\t" "v_exp_f32 %[o3], %[o3]\n\t" : [o0] "+a"(e.acc[2][10]), [o1] "+v"(tz[0][2]), [o2] "+v"(tz[1][2]), [o3] "+v"(tz[0][3]) : [i0] "v"(wf), [i1] "a"(e.tok[5][0][2]));
  asm volatile("v_mfma_f32_16x16x32_bf16 %[o0], %[i0], %[i1], %[o0]\n\t" "v_exp_f32 %[o1], %[o1]\n\t" "v_fma_f32 %[o2], %[o2], %[i2], %[i2]\n\t" : [o0] "+a"(e.acc[3][10]), [o1] "+v"(tz[1][3]), [o2] "+v"(tz[0][0]) : [i0] "v"(wf), [i1] "a"(e.tok[5][0][3]), [i2] "v"(sc.ri[2]));
  }
  asm volatile("s_waitcnt lgkmcnt(4)");
  { const u32x4 wf = __builtin_shufflevector(e.wlo[3], e.whi[3], 0, 1, 2, 3);
  asm volatile("v_mfma_f32_16x16x32_bf16 %[o0], %[i0], %[i1], %[o0]\n\t" "ds_read_b64_tr_b16 %[o1], %[i2] offset:49152\n\t" "v_fma_f32 %[o2], %[o2], %[i3], %[i3]\n\t" : [o0] "+a"(e.acc[0][11]), [o1] "=&v"(e.wlo[2]), [o2] "+v"(tz[1][0]) : [i0] "v"(wf), [i1] "a"(e.tok[5][0][0]), [i2] "v"(e.laddr[1][5]), [i3] "v"(sc.ri[3]));
  asm volatile("v_mfma_f32_16x16x32_bf16 %[o0], %[i0], %[i1], %[o0]\n\t" "ds_read_b64_tr_b16 %[o1], %[i2] offset:53248\n\t" "v_fma_f32 %[o2], %[o2], %[i3], %[i3]\n\t" : [o0] "+a"(e.acc[1][11]), [o1] "=&v"(e.whi[2]), [o2] "+v"(tz[0][1]) : [i0] "v"(wf), [i1] "a"(e.tok[5][0][1]), [i2] "v"(e.laddr[1][5]), [i3] "v"(sc.ri[2]));
  asm volatile("v_mfma_f32_16x16x32_bf16 %[o0], %[i0], %[i1], %[o0]\n\t" "v_fma_f32 %[o1], %[o1], %[i2], %[i2]\n\t" "v_fma_f32 %[o2], %[o2], %[i3], %[i3]\n\t" "v_fma_f32 %[o3], %[o3], %[i4], %[i4]\n\t" : [o0] "+a"(e.acc[2][11]), [o1] "+v"(tz[1][1]), [o2] "+v"(tz[0][2]), [o3] "+v"(tz[1][2]) : [i0] "v"(wf), [i1] "a"(e.tok[5][0][2]), [i2] "v"(sc.ri[3]), [i3] "v"(sc.ri[2]), [i4] "v"(sc.ri[3]));
  asm volatile("v_mfma_f32_16x16x32_bf16 %[o0], %[i0], %[i1], %[o0]\n\t" "v_fma_f32 %[o1], %[o1], %[i2], %[i2]\n\t" "v_fma_f32 %[o2], %[o2], %[i3], %[i3]\n\t" : [o0] "+a"(e.acc[3][11]), [o1] "+v"(tz[0][3]), [o2] "+v"(tz[1][3]) : [i0] "v"(wf), [i1] "a"(e.tok[5][0][3]), [i2] "v"(sc.ri[2]), [i3] "v"(sc.ri[3]));
  }
  asm volatile("s_waitcnt lgkmcnt(4)");
  { const u32x4 wf = __builtin_shufflevector(e.wlo[0], e.whi[0], 0, 1, 2, 3);
  asm volatile("v_mfma_f32_16x16x32_bf16 %[o0], %[i0], %[i1], %[o0]\n\t" "ds_read_b64_tr_b16 %[o1], %[i2] offset:57344\n\t" "v_rcp_f32 %[o2], %[o2]\n\t" : [o0] "+a"(e.acc[0][10]), [o1] "=&v"(e.wlo[3]), [o2] "+v"(tz[0][0]) : [i0] "v"(wf), [i1] "a"(e.tok[6][0][0]), [i2] "v"(e.laddr[1][5]));
  asm volatile("v_mfma_f32_16x16x32_bf16 %[o0], %[i0], %[i1], %[o0]\n\t" "ds_read_b64_tr_b16 %[o1], %[i2] offset:61440\n\t" "v_rcp_f32 %[o2], %[o2]\n\t" : [o0] "+a"(e.acc[1][10]), [o1] "=&v"(e.whi[3]), [o2] "+v"(tz[1][0]) : [i0] "v"(wf), [i1] "a"(e.tok[6][0][1]), [i2] "v"(e.laddr[1][5]));
  asm volatile("v_mfma_f32_16x16x32_bf16 %[o0], %[i0], %[i1], %[o0]\n\t" "v_rcp_f32 %[o1], %[o1]\n\t" "v_rcp_f32 %[o2], %[o2]\n\t" "v_rcp_f32 %[o3], %[o3]\n\t" : [o0] "+a"(e.acc[2][10]), [o1] "+v"(tz[0][1]), [o2] "+v"(tz[1][1]), [o3] "+v"(tz[0][2]) : [i0] "v"(wf), [i1] "a"(e.tok[6][0][2]));
  asm volatile("v_mfma_f32_16x16x32_bf16 %[o0], %[i0], %[i1], %[o0]\n\t" "v_rcp_f32 %[o1], %[o1]\n\t" "v_rcp_f32 %[o2], %[o2]\n\t" : [o0] "+a"(e.acc[3][10]), [o1] "+v"(tz[1][2]), [o2] "+v"(tz[0][3]) : [i0] "v"(wf), [i1] "a"(e.tok[6][0][3]));
  }
  asm volatile("s_waitcnt lgkmcnt(4)");
  { const u32x4 wf = __builtin_shufflevector(e.wlo[1], e.whi[1], 0, 1, 2, 3);
  asm volatile("v_mfma_f32_16x16x32_bf16 %[o0], %[i0], %[i1], %[o0]\n\t" "ds_read_b64_tr_b16 %[o1], %[i2] offset:16384\n\t" "v_rcp_f32 %[o2], %[o2]\n\t" : [o0] "+a"(e.acc[0][11]), [o1] "=&v"(e.wlo[0]), [o2] "+v"(tz[1][3]) : [i0] "v"(wf), [i1] "a"(e.tok[6][0][0]), [i2] "v"(e.laddr[0][6]));
  asm volatile("v_mfma_f32_16x16x32_bf16 %[o0], %[i0], %[i1], %[o0]\n\t" "ds_read_b64_tr_b16 %[o1], %[i2] offset:20480\n\t" "v_mul_f32 %[o2], %[i3], %[i4]\n\t" : [o0] "+a"(e.acc[1][11]), [o1] "=&v"(e.whi[0]), [o2] "=&v"(tw[0][0]) : [i0] "v"(wf), [i1] "a"(e.tok[6][0][1]), [i2] "v"(e.laddr[0][6]), [i3] "v"(e.acc[2][9][0]), [i4] "v"(tz[0][0]));
  asm volatile("v_mfma_f32_16x16x32_bf16 %[o0], %[i0], %[i1], %[o0]\n\t" "v_mul_f32 %[o1], %[i2], %[i3]\n\t" "v_mul_f32 %[o2], %[i4], %[i5]\n\t" "v_mul_f32 %[o3], %[i6], %[i7]\n\t" : [o0] "+a"(e.acc[2][11]), [o1] "=&v"(tw[1][0]), [o2] "=&v"(tw[0][1]), [o3] "=&v"(tw[1][1]) : [i0] "v"(wf), [i1] "a"(e.tok[6][0][2]), [i2] "v"(e.acc[3][9][0]), [i3] "v"(tz[1][0]), [i4] "v"(e.acc[2][9][1]), [i5] "v"(tz[0][1]), [i6] "v"(e.acc[3][9][1]), [i7] "v"(tz[1][1]));
  asm volatile("v_mfma_f32_16x16x32_bf16 %[o0], %[i0], %[i1], %[o0]\n\t" "v_mul_f32 %[o1], %[i2], %[i3]\n\t" "v_mul_f32 %[o2], %[i4], %[i5]\n\t" : [o0] "+a"(e.acc[3][11]), [o1] "=&v"(tw[0][2]), [o2] "=&v"(tw[1][2]) : [i0] "v"(wf), [i1] "a"(e.tok[6][0][3]), [i2] "v"(e.acc[2][9][2]), [i3] "v"(tz[0][2]), [i4] "v"(e.acc[3][9][2]), [i5] "v"(tz[1][2]));
  }
  asm volatile("s_waitcnt lgkmcnt(4)");
  { const u32x4 wf = __builtin_shufflevector(e.wlo[2], e.whi[2], 0, 1, 2, 3);
  asm volatile("v_mfma_f32_16x16x32_bf16 %[o0], %[i0], %[i1], %[o0]\n\t" "ds_read_b64_tr_b16 %[o1], %[i2] offset:24576\n\t" "v_mul_f32 %[o2], %[i3], %[i4]\n\t" : [o0] "+a"(e.acc[0][10]), [o1] "=&v"(e.wlo[1]), [o2] "=&v"(tw[0][3]) : [i0] "v"(wf), [i1] "a"(e.tok[7][0][0]), [i2] "v"(e.laddr[0][6]), [i3] "v"(e.acc[2][9][3]), [i4] "v"(tz[0][3]));
  asm volatile("v_mfma_f32_16x16x32_bf16 %[o0], %[i0], %[i1], %[o0]\n\t" "ds_read_b64_tr_b16 %[o1], %[i2] offset:28672\n\t" "v_mul_f32 %[o2], %[i3], %[i4]\n\t" : [o0] "+a"(e.acc[1][10]), [o1] "=&v"(e.whi[1]), [o2] "=&v"(tw[1][3]) : [i0] "v"(wf), [i1] "a"(e.tok[7][0][1]), [i2] "v"(e.laddr[0][6]), [i3] "v"(e.acc[3][9][3]), [i4] "v"(tz[1][3]));
  asm volatile("v_mfma_f32_16x16x32_bf16 %[o0], %[i0], %[i1], %[o0]\n\t" "v_cvt_pk_bf16_f32 %[o1], %[i2], %[i3]\n\t" "v_cvt_pk_bf16_f32 %[o2], %[i4], %[i5]\n\t" "v_cvt_pk_bf16_f32 %[o3], %[i6], %[i7]\n\t" : [o0] "+a"(e.acc[2][10]), [o1] "=&v"(pk[0][2][0]), [o2] "=&v"(pk[0][3][0]), [o3] "=&v"(pk[0][2][1]) : [i0] "v"(wf), [i1] "a"(e.tok[7][0][2]), [i2] "v"(tw[0][0]), [i3] "v"(tw[0][1]), [i4] "v"(tw[1][0]), [i5] "v"(tw[1][1]), [i6] "v"(tw[0][2]), [i7] "v"(tw[0][3]));
  asm volatile("v_mfma_f32_16x16x32_bf16 %[o0], %[i0], %[i1], %[o0]\n\t" "v_cvt_pk_bf16_f32 %[o1], %[i2], %[i3]\n\t" : [o0] "+a"(e.acc[3][10]), [o1] "=&v"(pk[0][3][1]) : [i0] "v"(wf), [i1] "a"(e.tok[7][0][3]), [i2] "v"(tw[1][2]), [i3] "v"(tw[1][3]));
  st128a(c.wsr, p0[1] + 0, so.pair[4], (u32x4){pk[1][2][0], pk[1][2][1], pk[0][2][0], pk[0][2][1]});
  }
  asm volatile("s_waitcnt lgkmcnt(4)");
  { const u32x4 wf = __builtin_shufflevector(e.wlo[3], e.whi[3], 0, 1, 2, 3);
  asm volatile("v_mfma_f32_16x16x32_bf16 %[o0], %[i0], %[i1], %[o0]\n\t" "ds_read_b64_tr_b16 %[o1], %[i2] offset:32768\n\t" : [o0] "+a"(e.acc[0][11]), [o1] "=&v"(e.wlo[2]) : [i0] "v"(wf), [i1] "a"(e.tok[7][0][0]), [i2] "v"(e.laddr[0][6]));
  st128a(c.wsr, p0[1] + 1024, so.pair[4], (u32x4){pk[1][3][0], pk[1][3][1], pk[0][3][0], pk[0][3][1]});
  asm volatile("v_mfma_f32_16x16x32_bf16 %[o0], %[i0], %[i1], %[o0]\n\t" "ds_read_b64_tr_b16 %[o1], %[i2] offset:36864\n\t" : [o0] "+a"(e.acc[1][11]), [o1] "=&v"(e.whi[2]) : [i0] "v"(wf), [i1] "a"(e.tok[7][0][1]), [i2] "v"(e.laddr[0][6]));
  asm volatile("v_mfma_f32_16x16x32_bf16 %[o0], %[i0], %[i1], %[o0]\n\t" : [o0] "+a"(e.acc[2][11]) : [i0] "v"(wf), [i1] "a"(e.tok[7][0][2]));
  asm volatile("v_mfma_f32_16x16x32_bf16 %[o0], %[i0], %[i1], %[o0]\n\t" : [o0] "+a"(e.acc[3][11]) : [i0] "v"(wf), [i1] "a"(e.tok[7][0][3]));
  }
  asm volatile("s_waitcnt lgkmcnt(4)");
  { const u32x4 wf = __builtin_shufflevector(e.wlo[0], e.whi[0], 0, 1, 2, 3);
  asm volatile("v_mfma_f32_16x16x32_bf16 %[o0], %[i0], %[i1], %[o0]\n\t" "ds_read_b64_tr_b16 %[o1], %[i2] offset:40960\n\t" "v_accvgpr_read_b32 %[o2], %[i3]\n\t" : [o0] "+a"(e.acc[0][12]), [o1] "=&v"(e.wlo[3]), [o2] "=&v"(gv[0][0]) : [i0] "v"(wf), [i1] "a"(e.tok[1][0][0]), [i2] "v"(e.laddr[0][6]), [i3] "a"(e.acc[0][10][0]));
  asm volatile("v_mfma_f32_16x16x32_bf16 %[o0], %[i0], %[i1], %[o0]\n\t" "ds_read_b64_tr_b16 %[o1], %[i2] offset:45056\n\t" "v_accvgpr_read_b32 %[o2], %[i3]\n\t" : [o0] "+a"(e.acc[1][12]), [o1] "=&v"(e.whi[3]), [o2] "=&v"(gv[1][0]) : [i0] "v"(wf), [i1] "a"(e.tok[1][0][1]), [i2] "v"(e.laddr[0][6]), [i3] "a"(e.acc[1][10][0]));
  asm volatile("v_mfma_f32_16x16x32_bf16 %[o0], %[i0], %[i1], %[o0]\n\t" "v_accvgpr_read_b32 %[o1], %[i2]\n\t" "v_accvgpr_read_b32 %[o2], %[i3]\n\t" "v_accvgpr_read_b32 %[o3], %[i4]\n\t" "v_accvgpr_read_b32 %[o4], %[i5]\n\t" : [o0] "+a"(e.acc[2][12]), [o1] "=&v"(gv[0][1]), [o2] "=&v"(gv[1][1]), [o3] "=&v"(gv[0][2]), [o4] "=&v"(gv[1][2]) : [i0] "v"(wf), [i1] "a"(e.tok[1][0][2]), [i2] "a"(e.acc[0][10][1]), [i3] "a"(e.acc[1][10][1]), [i4] "a"(e.acc[0][10][2]), [i5] "a"(e.acc[1][10][2]));
  asm volatile("v_mfma_f32_16x16x32_bf16 %[o0], %[i0], %[i1], %[o0]\n\t" "v_accvgpr_read_b32 %[o1], %[i2]\n\t" "v_accvgpr_read_b32 %[o2], %[i3]\n\t" "v_accvgpr_read_b32 %[o3], %[i4]\n\t" : [o0] "+a"(e.acc[3][12]), [o1] "=&v"(gv[0][3]), [o2] "=&v"(gv[1][3]), [o3] "=&v"(uv[0][0]) : [i0] "v"(wf), [i1] "a"(e.tok[1][0][3]), [i2] "a"(e.acc[0][10][3]), [i3] "a"(e.acc[1][10][3]), [i4] "a"(e.acc[0][11][0]));
  }
  asm volatile("s_waitcnt lgkmcnt(4)");
  { const u32x4 wf = __builtin_shufflevector(e.wlo[1], e.whi[1], 0, 1, 2, 3);
  asm volatile("v_mfma_f32_16x16x32_bf16 %[o0], %[i0], %[i1], %[o0]\n\t" "ds_read_b64_tr_b16 %[o1], %[i2] offset:49152\n\t" "v_accvgpr_read_b32 %[o2], %[i3]\n\t" : [o0] "+a"(e.acc[0][13]), [o1] "=&v"(e.wlo[0]), [o2] "=&v"(uv[1][0]) : [i0] "v"(wf), [i1] "a"(e.tok[1][0][0]), [i2] "v"(e.laddr[0][6]), [i3] "a"(e.acc[1][11][0]));
  asm volatile("v_mfma_f32_16x16x32_bf16 %[o0], %[i0], %[i1], %[o0]\n\t" "ds_read_b64_tr_b16 %[o1], %[i2] offset:53248\n\t" "v_accvgpr_read_b32 %[o2], %[i3]\n\t" : [o0] "+a"(e.acc[1][13]), [o1] "=&v"(e.whi[0]), [o2] "=&v"(uv[0][1]) : [i0] "v"(wf), [i1] "a"(e.tok[1][0][1]), [i2] "v"(e.laddr[0][6]), [i3] "a"(e.acc[0][11][1]));
  asm volatile("v_mfma_f32_16x16x32_bf16 %[o0], %[i0], %[i1], %[o0]\n\t" "v_accvgpr_read_b32 %[o1], %[i2]\n\t" "v_accvgpr_read_b32 %[o2], %[i3]\n\t" "v_accvgpr_read_b32 %[o3], %[i4]\n\t" "v_accvgpr_read_b32 %[o4], %[i5]\n\t" : [o0] "+a"(e.acc[2][13]), [o1] "=&v"(uv[1][1]), [o2] "=&v"(uv[0][2]), [o3] "=&v"(uv[1][2]), [o4] "=&v"(uv[0][3]) : [i0] "v"(wf), [i1] "a"(e.tok[1][0][2]), [i2] "a"(e.acc[1][11][1]), [i3] "a"(e.acc[0][11][2]), [i4] "a"(e.acc[1][11][2]), [i5] "a"(e.acc[0][11][3]));
  asm volatile("v_mfma_f32_16x16x32_bf16 %[o0], %[i0], %[i1], %[o0]\n\t" "v_accvgpr_read_b32 %[o1], %[i2]\n\t" "v_mul_f32 %[o2], %[i3], %[i4]\n\t" "v_mul_f32 %[o3], %[i5], %[i6]\n\t" : [o0] "+a"(e.acc[3][13]), [o1] "=&v"(uv[1][3]), [o2] "=&v"(tz[0][0]), [o3] "=&v"(tz[1][0]) : [i0] "v"(wf), [i1] "a"(e.tok[1][0][3]), [i2] "a"(e.acc[1][11][3]), [i3] "v"(gv[0][0]), [i4] "v"(sc.zr[0]), [i5] "v"(gv[1][0]), [i6] "v"(sc.zr[1]));
  }
  asm volatile("s_waitcnt lgkmcnt(4)");
  { const u32x4 wf = __builtin_shufflevector(e.wlo[2], e.whi[2], 0, 1, 2, 3);
  asm volatile("v_mfma_f32_16x16x32_bf16 %[o0], %[i0], %[i1], %[o0]\n\t" "ds_read_b64_tr_b16 %[o1], %[i2] offset:57344\n\t" "v_mul_f32 %[o2], %[i3], %[i4]\n\t" : [o0] "+a"(e.acc[0][12]), [o1] "=&v"(e.wlo[1]), [o2] "=&v"(tz[0][1]) : [i0] "v"(wf), [i1] "a"(e.tok[2][0][0]), [i2] "v"(e.laddr[0][6]), [i3] "v"(gv[0][1]), [i4] "v"(sc.zr[0]));
  asm volatile("v_mfma_f32_16x16x32_bf16 %[o0], %[i0], %[i1], %[o0]\n\t" "ds_read_b64_tr_b16 %[o1], %[i2] offset:61440\n\t" "v_mul_f32 %[o2], %[i3], %[i4]\n\t" : [o0] "+a"(e.acc[1][12]), [o1] "=&v"(e.whi[1]), [o2] "=&v"(tz[1][1]) : [i0] "v"(wf), [i1] "a"(e.tok[2][0][1]), [i2] "v"(e.laddr[0][6]), [i3] "v"(gv[1][1]), [i4] "v"(sc.zr[1]));
  asm volatile("v_mfma_f32_16x16x32_bf16 %[o0], %[i0], %[i1], %[o0]\n\t" "v_mul_f32 %[o1], %[i2], %[i3]\n\t" "v_mul_f32 %[o2], %[i4], %[i5]\n\t" "v_mul_f32 %[o3], %[i6], %[i7]\n\t" "v_mul_f32 %[o4], %[i8], %[i9]\n\t" : [o0] "+a"(e.acc[2][12]), [o1] "=&v"(tz[0][2]), [o2] "=&v"(tz[1][2]), [o3] "=&v"(tz[0][3]), [o4] "=&v"(tz[1][3]) : [i0] "v"(wf), [i1] "a"(e.tok[2][0][2]), [i2] "v"(gv[0][2]), [i3] "v"(sc.zr[0]), [i4] "v"(gv[1][2]), [i5] "v"(sc.zr[1]), [i6] "v"(gv[0][3]), [i7] "v"(sc.zr[0]), [i8] "v"(gv[1][3]), [i9] "v"(sc.zr[1]));
  asm volatile("v_mfma_f32_16x16x32_bf16 %[o0], %[i0], %[i1], %[o0]\n\t" "v_exp_f32 %[o1], %[o1]\n\t" "v_exp_f32 %[o2], %[o2]\n\t" "v_exp_f32 %[o3], %[o3]\n\t" : [o0] "+a"(e.acc[3][12]), [o1] "+v"(tz[0][0]), [o2] "+v"(tz[1][0]), [o3] "+v"(tz[0][1]) : [i0] "v"(wf), [i1] "a"(e.tok[2][0][3]));
  }
  asm volatile("s_waitcnt lgkmcnt(4)");
  { const u32x4 wf = __builtin_shufflevector(e.wlo[3], e.whi[3], 0, 1, 2, 3);
  asm volatile("v_mfma_f32_16x16x32_bf16 %[o0], %[i0], %[i1], %[o0]\n\t" "ds_read_b64_tr_b16 %[o1], %[i2] offset:0\n\t" "v_exp_f32 %[o2], %[o2]\n\t" : [o0] "+a"(e.acc[0][13]), [o1] "=&v"(e.wlo[2]), [o2] "+v"(tz[1][1]) : [i0] "v"(wf), [i1] "a"(e.tok[2][0][0]), [i2] "v"(e.laddr[1][6]));
  asm volatile("v_mfma_f32_16x16x32_bf16 %[o0], %[i0], %[i1], %[o0]\n\t" "ds_read_b64_tr_b16 %[o1], %[i2] offset:4096\n\t" "v_exp_f32 %[o2], %[o2]\n\t" : [o0] "+a"(e.acc[1][13]), [o1] "=&v"(e.whi[2]), [o2] "+v"(tz[0][2]) : [i0] "v"(wf), [i1] "a"(e.tok[2][0][1]), [i2] "v"(e.laddr[1][6]));
  asm volatile("v_mfma_f32_16x16x32_bf16 %[o0], %[i0], %[i1], %[o0]\n\t" "v_exp_f32 %[o1], %[o1]\n\t" "v_exp_f32 %[o2], %[o2]\n\t" "v_exp_f32 %[o3], %[o3]\n\t" "v_fma_f32 %[o4], %[o4], %[i2], %[i2]\n\t" : [o0] "+a"(e.acc[2][13]), [o1] "+v"(tz[1][2]), [o2] "+v"(tz[0][3]), [o3] "+v"(tz[1][3]), [o4] "+v"(tz[0][0]) : [i0] "v"(wf), [i1] "a"(e.tok[2][0][2]), [i2] "v"(sc.ri[0]));
  asm volatile("v_mfma_f32_16x16x32_bf16 %[o0], %[i0], %[i1], %[o0]\n\t" "v_fma_f32 %[o1], %[o1], %[i2], %[i2]\n\t" "v_fma_f32 %[o2], %[o2], %[i3], %[i3]\n\t" "v_fma_f32 %[o3], %[o3], %[i4], %[i4]\n\t" : [o0] "+a"(e.acc[3][13]), [o1] "+v"(tz[1][0]), [o2] "+v"(tz[0][1]), [o3] "+v"(tz[1][1]) : [i0] "v"(wf), [i1] "a"(e.tok[2][0][3]), [i2] "v"(sc.ri[1]), [i3] "v"(sc.ri[0]), [i4] "v"(sc.ri[1]));
  }
  asm volatile("s_waitcnt lgkmcnt(4)");
  { const u32x4 wf = __builtin_shufflevector(e.wlo[0], e.whi[0], 0, 1, 2, 3);
  asm volatile("v_mfma_f32_16x16x32_bf16 %[o0], %[i0], %[i1], %[o0]\n\t" "ds_read_b64_tr_b16 %[o1], %[i2] offset:8192\n\t" "v_fma_f32 %[o2], %[o2], %[i3], %[i3]\n\t" : [o0] "+a"(e.acc[0][12]), [o1] "=&v"(e.wlo[3]), [o2] "+v"(tz[0][2]) : [i0] "v"(wf), [i1] "a"(e.tok[3][0][0]), [i2] "v"(e.laddr[1][6]), [i3] "v"(sc.ri[0]));
  asm volatile("v_mfma_f32_16x16x32_bf16 %[o0], %[i0], %[i1], %[o0]\n\t" "ds_read_b64_tr_b16 %[o1], %[i2] offset:12288\n\t" "v_fma_f32 %[o2], %[o2], %[i3], %[i3]\n\t" : [o0] "+a"(e.acc[1][12]), [o1] "=&v"(e.whi[3]), [o2] "+v"(tz[1][2]) : [i0] "v"(wf), [i1] "a"(e.tok[3][0][1]), [i2] "v"(e.laddr[1][6]), [i3] "v"(sc.ri[1]));
  asm volatile("v_mfma_f32_16x16x32_bf16 %[o0], %[i0], %[i1], %[o0]\n\t" "v_fma_f32 %[o1], %[o1], %[i2], %[i2]\n\t" "v_fma_f32 %[o2], %[o2], %[i3], %[i3]\n\t" "v_rcp_f32 %[o3], %[o3]\n\t" "v_rcp_f32 %[o4], %[o4]\n\t" : [o0] "+a"(e.acc[2][12]), [o1] "+v"(tz[0][3]), [o2] "+v"(tz[1][3]), [o3] "+v"(tz[0][0]), [o4] "+v"(tz[1][0]) : [i0] "v"(wf), [i1] "a"(e.tok[3][0][2]), [i2] "v"(sc.ri[0]), [i3] "v"(sc.ri[1]));
  asm volatile("v_mfma_f32_16x16x32_bf16 %[o0], %[i0], %[i1], %[o0]\n\t" "v_rcp_f32 %[o1], %[o1]\n\t" "v_rcp_f32 %[o2], %[o2]\n\t" "v_rcp_f32 %[o3], %[o3]\n\t" : [o0] "+a"(e.acc[3][12]), [o1] "+v"(tz[0][1]), [o2] "+v"(tz[1][1]), [o3] "+v"(tz[0][2]) : [i0] "v"(wf), [i1] "a"(e.tok[3][0][3]));
  }
  asm volatile("s_waitcnt lgkmcnt(4)");
  { const u32x4 wf = __builtin_shufflevector(e.wlo[1], e.whi[1], 0, 1, 2, 3);
  asm volatile("v_mfma_f32_16x16x32_bf16 %[o0], %[i0], %[i1], %[o0]\n\t" "ds_read_b64_tr_b16 %[o1], %[i2] offset:16384\n\t" "v_rcp_f32 %[o2], %[o2]\n\t" : [o0] "+a"(e.acc[0][13]), [o1] "=&v"(e.wlo[0]), [o2] "+v"(tz[1][2]) : [i0] "v"(wf), [i1] "a"(e.tok[3][0][0]), [i2] "v"(e.laddr[1][6]));
  asm volatile("v_mfma_f32_16x16x32_bf16 %[o0], %[i0], %[i1], %[o0]\n\t" "ds_read_b64_tr_b16 %[o1], %[i2] offset:20480\n\t" "v_rcp_f32 %[o2], %[o2]\n\t" : [o0] "+a"(e.acc[1][13]), [o1] "=&v"(e.whi[0]), [o2] "+v"(tz[0][3]) : [i0] "v"(wf), [i1] "a"(e.tok[3][0][1]), [i2] "v"(e.laddr[1][6]));
  asm volatile("v_mfma_f32_16x16x32_bf16 %[o0], %[i0], %[i1], %[o0]\n\t" "v_rcp_f32 %[o1], %[o1]\n\t" "v_mul_f32 %[o2], %[i2], %[i3]\n\t" "v_mul_f32 %[o3], %[i4], %[i5]\n\t" "v_mul_f32 %[o4], %[i6], %[i7]\n\t" : [o0] "+a"(e.acc[2][13]), [o1] "+v"(tz[1][3]), [o2] "=&v"(tw[0][0]), [o3] "=&v"(tw[1][0]), [o4] "=&v"(tw[0][1]) : [i0] "v"(wf), [i1] "a"(e.tok[3][0][2]), [i2] "v"(uv[0][0]), [i3] "v"(tz[0][0]), [i4] "v"(uv[1][0]), [i5] "v"(tz[1][0]), [i6] "v"(uv[0][1]), [i7] "v"(tz[0][1]));
  asm volatile("v_mfma_f32_16x16x32_bf16 %[o0], %[i0], %[i1], %[o0]\n\t" "v_mul_f32 %[o1], %[i2], %[i3]\n\t" "v_mul_f32 %[o2], %[i4], %[i5]\n\t" "v_mul_f32 %[o3], %[i6], %[i7]\n\t" : [o0] "+a"(e.acc[3][13]), [o1] "=&v"(tw[1][1]), [o2] "=&v"(tw[0][2]), [o3] "=&v"(tw[1][2]) : [i0] "v"(wf), [i1] "a"(e.tok[3][0][3]), [i2] "v"(uv[1][1]), [i3] "v"(tz[1][1]), [i4] "v"(uv[0][2]), [i5] "v"(tz[0][2]), [i6] "v"(uv[1][2]), [i7] "v"(tz[1][2]));
  }
  asm volatile("s_waitcnt lgkmcnt(4)");
  { const u32x4 wf = __builtin_shufflevector(e.wlo[2], e.whi[2], 0, 1, 2, 3);
  asm volatile("v_mfma_f32_16x16x32_bf16 %[o0], %[i0], %[i1], %[o0]\n\t" "ds_read_b64_tr_b16 %[o1], %[i2] offset:24576\n\t" "v_mul_f32 %[o2], %[i3], %[i4]\n\t" : [o0] "+a"(e.acc[0][12]), [o1] "=&v"(e.wlo[1]), [o2] "=&v"(tw[0][3]) : [i0] "v"(wf), [i1] "a"(e.tok[4][0][0]), [i2] "v"(e.laddr[1][6]), [i3] "v"(uv[0][3]), [i4] "v"(tz[0][3]));
  asm volatile("v_mfma_f32_16x16x32_bf16 %[o0], %[i0], %[i1], %[o0]\n\t" "ds_read_b64_tr_b16 %[o1], %[i2] offset:28672\n\t" "v_mul_f32 %[o2], %[i3], %[i4]\n\t" : [o0] "+a"(e.acc[1][12]), [o1] "=&v"(e.whi[1]), [o2] "=&v"(tw[1][3]) : [i0] "v"(wf), [i1] "a"(e.tok[4][0][1]), [i2] "v"(e.laddr[1][6]), [i3] "v"(uv[1][3]), [i4] "v"(tz[1][3]));
  asm volatile("v_mfma_f32_16x16x32_bf16 %[o0], %[i0], %[i1], %[o0]\n\t" "v_cvt_pk_bf16_f32 %[o1], %[i2], %[i3]\n\t" "v_cvt_pk_bf16_f32 %[o2], %[i4], %[i5]\n\t" "v_cvt_pk_bf16_f32 %[o3], %[i6], %[i7]\n\t" "v_cvt_pk_bf16_f32 %[o4], %[i8], %[i9]\n\t" : [o0] "+a"(e.acc[2][12]), [o1] "=&v"(pk[1][0][0]), [o2] "=&v"(pk[1][1][0]), [o3] "=&v"(pk[1][0][1]), [o4] "=&v"(pk[1][1][1]) : [i0] "v"(wf), [i1] "a"(e.tok[4][0][2]), [i2] "v"(tw[0][0]), [i3] "v"(tw[0][1]), [i4] "v"(tw[1][0]), [i5] "v"(tw[1][1]), [i6] "v"(tw[0][2]), [i7] "v"(tw[0][3]), [i8] "v"(tw[1][2]), [i9] "v"(tw[1][3]));
  asm volatile("v_mfma_f32_16x16x32_bf16 %[o0], %[i0], %[i1], %[o0]\n\t" : [o0] "+a"(e.acc[3][12]) : [i0] "v"(wf), [i1] "a"(e.tok[4][0][3]));
  st128a(c.wsr, p0[0] + 0, so.pair[5], (u32x4){pk[0][0][0], pk[0][0][1], pk[1][0][0], pk[1][0][1]});
  st128a(c.wsr, p0[0] + 1024, so.pair[5], (u32x4){pk[0][1][0], pk[0][1][1], pk[1][1][0], pk[1][1][1]});
  asm volatile("v_accvgpr_read_b32 %[o0], %[i0]\n\t" : [o0] "=&v"(gv[0][0]) : [i0] "a"(e.acc[2][10][0]));
  }
  asm volatile("s_waitcnt lgkmcnt(4)");
  { const u32x4 wf = __builtin_shufflevector(e.wlo[3], e.whi[3], 0, 1, 2, 3);
  asm volatile("v_mfma_f32_16x16x32_bf16 %[o0], %[i0], %[i1], %[o0]\n\t" "ds_read_b64_tr_b16 %[o1], %[i2] offset:32768\n\t" "v_accvgpr_read_b32 %[o2], %[i3]\n\t" : [o0] "+a"(e.acc[0][13]), [o1] "=&v"(e.wlo[2]), [o2] "=&v"(gv[1][0]) : [i0] "v"(wf), [i1] "a"(e.tok[4][0][0]), [i2] "v"(e.laddr[1][6]), [i3] "a"(e.acc[3][10][0]));
  asm volatile("v_mfma_f32_16x16x32_bf16 %[o0], %[i0], %[i1], %[o0]\n\t" "ds_read_b64_tr_b16 %[o1], %[i2] offset:36864\n\t" "v_accvgpr_read_b32 %[o2], %[i3]\n\t" : [o0] "+a"(e.acc[1][13]), [o1] "=&v"(e.whi[2]), [o2] "=&v"(gv[0][1]) : [i0] "v"(wf), [i1] "a"(e.tok[4][0][1]), [i2] "v"(e.laddr[1][6]), [i3] "a"(e.acc[2][10][1]));
  asm volatile("v_mfma_f32_16x16x32_bf16 %[o0], %[i0], %[i1], %[o0]\n\t" "v_accvgpr_read_b32 %[o1], %[i2]\n\t" "v_accvgpr_read_b32 %[o2], %[i3]\n\t" "v_accvgpr_read_b32 %[o3], %[i4]\n\t" "v_accvgpr_read_b32 %[o4], %[i5]\n\t" : [o0] "+a"(e.acc[2][13]), [o1] "=&v"(gv[1][1]), [o2] "=&v"(gv[0][2]), [o3] "=&v"(gv[1][2]), [o4] "=&v"(gv[0][3]) : [i0] "v"(wf), [i1] "a"(e.tok[4][0][2]), [i2] "a"(e.acc[3][10][1]), [i3] "a"(e.acc[2][10][2]), [i4] "a"(e.acc[3][10][2]), [i5] "a"(e.acc[2][10][3]));
  asm volatile("v_mfma_f32_16x16x32_bf16 %[o0], %[i0], %[i1], %[o0]\n\t" "v_accvgpr_read_b32 %[o1], %[i2]\n\t" "v_accvgpr_read_b32 %[o2], %[i3]\n\t" "v_accvgpr_read_b32 %[o3], %[i4]\n\t" : [o0] "+a"(e.acc[3][13]), [o1] "=&v"(gv[1][3]), [o2] "=&v"(uv[0][0]), [o3] "=&v"(uv[1][0]) : [i0] "v"(wf), [i1] "a"(e.tok[4][0][3]), [i2] "a"(e.acc[3][10][3]), [i3] "a"(e.acc[2][11][0]), [i4] "a"(e.acc[3][11][0]));
  }
  asm volatile("s_waitcnt lgkmcnt(4)");
  { const u32x4 wf = __builtin_shufflevector(e.wlo[0], e.whi[0], 0, 1, 2, 3);
  asm volatile("v_mfma_f32_16x16x32_bf16 %[o0], %[i0], %[i1], %[o0]\n\t" "ds_read_b64_tr_b16 %[o1], %[i2] offset:40960\n\t" "v_accvgpr_read_b32 %[o2], %[i3]\n\t" : [o0] "+a"(e.acc[0][12]), [o1] "=&v"(e.wlo[3]), [o2] "=&v"(uv[0][1]) : [i0] "v"(wf), [i1] "a"(e.tok[5][0][0]), [i2] "v"(e.laddr[1][6]), [i3] "a"(e.acc[2][11][1]));
  asm volatile("v_mfma_f32_16x16x32_bf16 %[o0], %[i0], %[i1], %[o0]\n\t" "ds_read_b64_tr_b16 %[o1], %[i2] offset:45056\n\t" "v_accvgpr_read_b32 %[o2], %[i3]\n\t" : [o0] "+a"(e.acc[1][12]), [o1] "=&v"(e.whi[3]), [o2] "=&v"(uv[1][1]) : [i0] "v"(wf), [i1] "a"(e.tok[5][0][1]), [i2] "v"(e.laddr[1][6]), [i3] "a"(e.acc[3][11][1]));
  asm volatile("v_mfma_f32_16x16x32_bf16 %[o0], %[i0], %[i1], %[o0]\n\t" "v_accvgpr_read_b32 %[o1], %[i2]\n\t" "v_accvgpr_read_b32 %[o2], %[i3]\n\t" "v_accvgpr_read_b32 %[o3], %[i4]\n\t" "v_accvgpr_read_b32 %[o4], %[i5]\n\t" : [o0] "+a"(e.acc[2][12]), [o1] "=&v"(uv[0][2]), [o2] "=&v"(uv[1][2]), [o3] "=&v"(uv[0][3]), [o4] "=&v"(uv[1][3]) : [i0] "v"(wf), [i1] "a"(e.tok[5][0][2]), [i2] "a"(e.acc[2][11][2]), [i3] "a"(e.acc[3][11][2]), [i4] "a"(e.acc[2][11][3]), [i5] "a"(e.acc[3][11][3]));
  asm volatile("v_mfma_f32_16x16x32_bf16 %[o0], %[i0], %[i1], %[o0]\n\t" "v_mul_f32 %[o1], %[i2], %[i3]\n\t" "v_mul_f32 %[o2], %[i4], %[i5]\n\t" "v_mul_f32 %[o3], %[i6], %[i7]\n\t" : [o0] "+a"(e.acc[3][12]), [o1] "=&v"(tz[0][0]), [o2] "=&v"(tz[1][0]), [o3] "=&v"(tz[0][1]) : [i0] "v"(wf), [i1] "a"(e.tok[5][0][3]), [i2] "v"(gv[0][0]), [i3] "v"(sc.zr[2]), [i4] "v"(gv[1][0]), [i5] "v"(sc.zr[3]), [i6] "v"(gv[0][1]), [i7] "v"(sc.zr[2]));
  }
  asm volatile("s_waitcnt lgkmcnt(4)");
  { const u32x4 wf = __builtin_shufflevector(e.wlo[1], e.whi[1], 0, 1, 2, 3);
  asm volatile("v_mfma_f32_16x16x32_bf16 %[o0], %[i0], %[i1], %[o0]\n\t" "ds_read_b64_tr_b16 %[o1], %[i2] offset:49152\n\t" "v_mul_f32 %[o2], %[i3], %[i4]\n\t" : [o0] "+a"(e.acc[0][13]), [o1] "=&v"(e.wlo[0]), [o2] "=&v"(tz[1][1]) : [i0] "v"(wf), [i1] "a"(e.tok[5][0][0]), [i2] "v"(e.laddr[1][6]), [i3] "v"(gv[1][1]), [i4] "v"(sc.zr[3]));
  asm volatile("v_mfma_f32_16x16x32_bf16 %[o0], %[i0], %[i1], %[o0]\n\t" "ds_read_b64_tr_b16 %[o1], %[i2] offset:53248\n\t" "v_mul_f32 %[o2], %[i3], %[i4]\n\t" : [o0] "+a"(e.acc[1][13]), [o1] "=&v"(e.whi[0]), [o2] "=&v"(tz[0][2]) : [i0] "v"(wf), [i1] "a"(e.tok[5][0][1]), [i2] "v"(e.laddr[1][6]), [i3] "v"(gv[0][2]), [i4] "v"(sc.zr[2]));
  asm volatile("v_mfma_f32_16x16x32_bf16 %[o0], %[i0], %[i1], %[o0]\n\t" "v_mul_f32 %[o1], %[i2], %[i3]\n\t" "v_mul_f32 %[o2], %[i4], %[i5]\n\t" "v_mul_f32 %[o3], %[i6], %[i7]\n\t" "v_exp_f32 %[o4], %[o4]\n\t" : [o0] "+a"(e.acc[2][13]), [o1] "=&v"(tz[1][2]), [o2] "=&v"(tz[0][3]), [o3] "=&v"(tz[1][3]), [o4] "+v"(tz[0][0]) : [i0] "v"(wf), [i1] "a"(e.tok[5][0][2]), [i2] "v"(gv[1][2]), [i3] "v"(sc.zr[3]), [i4] "v"(gv[0][3]), [i5] "v"(sc.zr[2]), [i6] "v"(gv[1][3]), [i7] "v"(sc.zr[3]));
  asm volatile("v_mfma_f32_16x16x32_bf16 %[o0], %[i0], %[i1], %[o0]\n\t" "v_exp_f32 %[o1], %[o1]\n\t" "v_exp_f32 %[o2], %[o2]\n\t" "v_exp_f32 %[o3], %[o3]\n\t" : [o0] "+a"(e.acc[3][13]), [o1] "+v"(tz[1][0]), [o2] "+v"(tz[0][1]), [o3] "+v"(tz[1][1]) : [i0] "v"(wf), [i1] "a"(e.tok[5][0][3]));
  }
  asm volatile("s_waitcnt lgkmcnt(4)");
  { const u32x4 wf = __builtin_shufflevector(e.wlo[2], e.whi[2], 0, 1, 2, 3);
  asm volatile("v_mfma_f32_16x16x32_bf16 %[o0], %[i0], %[i1], %[o0]\n\t" "ds_read_b64_tr_b16 %[o1], %[i2] offset:57344\n\t" "v_exp_f32 %[o2], %[o2]\n\t" : [o0] "+a"(e.acc[0][12]), [o1] "=&v"(e.wlo[1]), [o2] "+v"(tz[0][2]) : [i0] "v"(wf), [i1] "a"(e.tok[6][0][0]), [i2] "v"(e.laddr[1][6]));
  asm volatile("v_mfma_f32_16x16x32_bf16 %[o0], %[i0], %[i1], %[o0]\n\t" "ds_read_b64_tr_b16 %[o1], %[i2] offset:61440\n\t" "v_exp_f32 %[o2], %[o2]\n\t" : [o0] "+a"(e.acc[1][12]), [o1] "=&v"(e.whi[1]), [o2] "+v"(tz[1][2]) : [i0] "v"(wf), [i1] "a"(e.tok[6][0][1]), [i2] "v"(e.laddr[1][6]));
  asm volatile("v_mfma_f32_16x16x32_bf16 %[o0], %[i0], %[i1], %[o0]\n\t" "v_exp_f32 %[o1], %[o1]\n\t" "v_exp_f32 %[o2], %[o2]\n\t" "v_fma_f32 %[o3], %[o3], %[i2], %[i2]\n\t" "v_fma_f32 %[o4], %[o4], %[i3], %[i3]\n\t" : [o0] "+a"(e.acc[2][12]), [o1] "+v"(tz[0][3]), [o2] "+v"(tz[1][3]), [o3] "+v"(tz[0][0]), [o4] "+v"(tz[1][0]) : [i0] "v"(wf), [i1] "a"(e.tok[6][0][2]), [i2] "v"(sc.ri[2]), [i3] "v"(sc.ri[3]));
  asm volatile("v_mfma_f32_16x16x32_bf16 %[o0], %[i0], %[i1], %[o0]\n\t" "v_fma_f32 %[o1], %[o1], %[i2], %[i2]\n\t" "v_fma_f32 %[o2], %[o2], %[i3], %[i3]\n\t" "v_fma_f32 %[o3], %[o3], %[i4], %[i4]\n\t" : [o0] "+a"(e.acc[3][12]), [o1] "+v"(tz[0][1]), [o2] "+v"(tz[1][1]), [o3] "+v"(tz[0][2]) : [i0] "v"(wf), [i1] "a"(e.tok[6][0][3]), [i2] "v"(sc.ri[2]), [i3] "v"(sc.ri[3]), [i4] "v"(sc.ri[2]));
  }
  asm volatile("s_waitcnt lgkmcnt(4)");
  { const u32x4 wf = __builtin_shufflevector(e.wlo[3], e.whi[3], 0, 1, 2, 3);
  asm volatile("v_mfma_f32_16x16x32_bf16 %[o0], %[i0], %[i1], %[o0]\n\t" "v_fma_f32 %[o1], %[o1], %[i2], %[i2]\n\t" : [o0] "+a"(e.acc[0][13]), [o1] "+v"(tz[1][2]) : [i0] "v"(wf), [i1] "a"(e.tok[6][0][0]), [i2] "v"(sc.ri[3]));
  asm volatile("v_mfma_f32_16x16x32_bf16 %[o0], %[i0], %[i1], %[o0]\n\t" "v_fma_f32 %[o1], %[o1], %[i2], %[i2]\n\t" : [o0] "+a"(e.acc[1][13]), [o1] "+v"(tz[0][3]) : [i0] "v"(wf), [i1] "a"(e.tok[6][0][1]), [i2] "v"(sc.ri[2]));
  asm volatile("v_mfma_f32_16x16x32_bf16 %[o0], %[i0], %[i1], %[o0]\n\t" "v_fma_f32 %[o1], %[o1], %[i2], %[i2]\n\t" "v_rcp_f32 %[o2], %[o2]\n\t" "v_rcp_f32 %[o3], %[o3]\n\t" "v_rcp_f32 %[o4], %[o4]\n\t" : [o0] "+a"(e.acc[2][13]), [o1] "+v"(tz[1][3]), [o2] "+v"(tz[0][0]), [o3] "+v"(tz[1][0]), [o4] "+v"(tz[0][1]) : [i0] "v"(wf), [i1] "a"(e.tok[6][0][2]), [i2] "v"(sc.ri[3]));
  asm volatile("v_mfma_f32_16x16x32_bf16 %[o0], %[i0], %[i1], %[o0]\n\t" "v_rcp_f32 %[o1], %[o1]\n\t" "v_rcp_f32 %[o2], %[o2]\n\t" "v_rcp_f32 %[o3], %[o3]\n\t" : [o0] "+a"(e.acc[3][13]), [o1] "+v"(tz[1][1]), [o2] "+v"(tz[0][2]), [o3] "+v"(tz[1][2]) : [i0] "v"(wf), [i1] "a"(e.tok[6][0][3]));
  }
  asm volatile("s_waitcnt lgkmcnt(2)");
  { const u32x4 wf = __builtin_shufflevector(e.wlo[0], e.whi[0], 0, 1, 2, 3);
  asm volatile("v_mfma_f32_16x16x32_bf16 %[o0], %[i0], %[i1], %[o0]\n\t" "v_rcp_f32 %[o1], %[o1]\n\t" : [o0] "+a"(e.acc[0][12]), [o1] "+v"(tz[0][3]) : [i0] "v"(wf), [i1] "a"(e.tok[7][0][0]));
  asm volatile("v_mfma_f32_16x16x32_bf16 %[o0], %[i0], %[i1], %[o0]\n\t" "v_rcp_f32 %[o1], %[o1]\n\t" : [o0] "+a"(e.acc[1][12]), [o1] "+v"(tz[1][3]) : [i0] "v"(wf), [i1] "a"(e.tok[7][0][1]));
  asm volatile("v_mfma_f32_16x16x32_bf16 %[o0], %[i0], %[i1], %[o0]\n\t" "v_mul_f32 %[o1], %[i2], %[i3]\n\t" "v_mul_f32 %[o2], %[i4], %[i5]\n\t" "v_mul_f32 %[o3], %[i6], %[i7]\n\t" "v_mul_f32 %[o4], %[i8], %[i9]\n\t" : [o0] "+a"(e.acc[2][12]), [o1] "=&v"(tw[0][0]), [o2] "=&v"(tw[1][0]), [o3] "=&v"(tw[0][1]), [o4] "=&v"(tw[1][1]) : [i0] "v"(wf), [i1] "a"(e.tok[7][0][2]), [i2] "v"(uv[0][0]), [i3] "v"(tz[0][0]), [i4] "v"(uv[1][0]), [i5] "v"(tz[1][0]), [i6] "v"(uv[0][1]), [i7] "v"(tz[0][1]), [i8] "v"(uv[1][1]), [i9] "v"(tz[1][1]));
  asm volatile("v_mfma_f32_16x16x32_bf16 %[o0], %[i0], %[i1], %[o0]\n\t" "v_mul_f32 %[o1], %[i2], %[i3]\n\t" "v_mul_f32 %[o2], %[i4], %[i5]\n\t" "v_mul_f32 %[o3], %[i6], %[i7]\n\t" : [o0] "+a"(e.acc[3][12]), [o1] "=&v"(tw[0][2]), [o2] "=&v"(tw[1][2]), [o3] "=&v"(tw[0][3]) : [i0] "v"(wf), [i1] "a"(e.tok[7][0][3]), [i2] "v"(uv[0][2]), [i3] "v"(tz[0][2]), [i4] "v"(uv[1][2]), [i5] "v"(tz[1][2]), [i6] "v"(uv[0][3]), [i7] "v"(tz[0][3]));
  }
  asm volatile("s_waitcnt lgkmcnt(0)");
  { const u32x4 wf = __builtin_shufflevector(e.wlo[1], e.whi[1], 0, 1, 2, 3);
  asm volatile("v_mfma_f32_16x16x32_bf16 %[o0], %[i0], %[i1], %[o0]\n\t" "v_mul_f32 %[o1], %[i2], %[i3]\n\t" : [o0] "+a"(e.acc[0][13]), [o1] "=&v"(tw[1][3]) : [i0] "v"(wf), [i1] "a"(e.tok[7][0][0]), [i2] "v"(uv[1][3]), [i3] "v"(tz[1][3]));
  asm volatile("v_mfma_f32_16x16x32_bf16 %[o0], %[i0], %[i1], %[o0]\n\t" "v_cvt_pk_bf16_f32 %[o1], %[i2], %[i3]\n\t" : [o0] "+a"(e.acc[1][13]), [o1] "=&v"(pk[1][2][0]) : [i0] "v"(wf), [i1] "a"(e.tok[7][0][1]), [i2] "v"(tw[0][0]), [i3] "v"(tw[0][1]));
  asm volatile("v_mfma_f32_16x16x32_bf16 %[o0], %[i0], %[i1], %[o0]\n\t" "v_cvt_pk_bf16_f32 %[o1], %[i2], %[i3]\n\t" "v_cvt_pk_bf16_f32 %[o2], %[i4], %[i5]\n\t" "v_cvt_pk_bf16_f32 %[o3], %[i6], %[i7]\n\t" : [o0] "+a"(e.acc[2][13]), [o1] "=&v"(pk[1][3][0]), [o2] "=&v"(pk[1][2][1]), [o3] "=&v"(pk[1][3][1]) : [i0] "v"(wf), [i1] "a"(e.tok[7][0][2]), [i2] "v"(tw[1][0]), [i3] "v"(tw[1][1]), [i4] "v"(tw[0][2]), [i5] "v"(tw[0][3]), [i6] "v"(tw[1][2]), [i7] "v"(tw[1][3]));
  asm volatile("v_mfma_f32_16x16x32_bf16 %[o0], %[i0], %[i1], %[o0]\n\t" : [o0] "+a"(e.acc[3][13]) : [i0] "v"(wf), [i1] "a"(e.tok[7][0][3]));
  st128a(c.wsr, p0[1] + 0, so.pair[5], (u32x4){pk[0][2][0], pk[0][2][1], pk[1][2][0], pk[1][2][1]});
  st128a(c.wsr, p0[1] + 1024, so.pair[5], (u32x4){pk[0][3][0], pk[0][3][1], pk[1][3][0], pk[1][3][1]});
  }
  asm volatile("s_waitcnt lgkmcnt(0)\n\ts_nop 7\n\ts_nop 7");
  asm volatile("v_accvgpr_read_b32 %[o0], %[i0]\n\t" "v_accvgpr_read_b32 %[o1], %[i1]\n\t" "v_accvgpr_read_b32 %[o2], %[i2]\n\t" "v_accvgpr_read_b32 %[o3], %[i3]\n\t" "v_accvgpr_read_b32 %[o4], %[i4]\n\t" "v_accvgpr_read_b32 %[o5], %[i5]\n\t" "v_accvgpr_read_b32 %[o6], %[i6]\n\t" "v_accvgpr_read_b32 %[o7], %[i7]\n\t" "v_accvgpr_read_b32 %[o8], %[i8]\n\t" "v_accvgpr_read_b32 %[o9], %[i9]\n\t" "v_accvgpr_read_b32 %[o10], %[i10]\n\t" "v_accvgpr_read_b32 %[o11], %[i11]\n\t" "v_accvgpr_read_b32 %[o12], %[i12]\n\t" "v_accvgpr_read_b32 %[o13], %[i13]\n\t" "v_accvgpr_read_b32 %[o14], %[i14]\n\t" "v_accvgpr_read_b32 %[o15], %[i15]\n\t" : [o0] "=&v"(gv[0][0]), [o1] "=&v"(gv[1][0]), [o2] "=&v"(gv[0][1]), [o3] "=&v"(gv[1][1]), [o4] "=&v"(gv[0][2]), [o5] "=&v"(gv[1][2]), [o6] "=&v"(gv[0][3]), [o7] "=&v"(gv[1][3]), [o8] "=&v"(uv[0][0]), [o9] "=&v"(uv[1][0]), [o10] "=&v"(uv[0][1]), [o11] "=&v"(uv[1][1]), [o12] "=&v"(uv[0][2]), [o13] "=&v"(uv[1][2]), [o14] "=&v"(uv[0][3]), [o15] "=&v"(uv[1][3]) : [i0] "a"(e.acc[0][12][0]), [i1] "a"(e.acc[1][12][0]), [i2] "a"(e.acc[0][12][1]), [i3] "a"(e.acc[1][12][1]), [i4] "a"(e.acc[0][12][2]), [i5] "a"(e.acc[1][12][2]), [i6] "a"(e.acc[0][12][3]), [i7] "a"(e.acc[1][12][3]), [i8] "a"(e.acc[0][13][0]), [i9] "a"(e.acc[1][13][0]), [i10] "a"(e.acc[0][13][1]), [i11] "a"(e.acc[1][13][1]), [i12] "a"(e.acc[0][13][2]), [i13] "a"(e.acc[1][13][2]), [i14] "a"(e.acc[0][13][3]), [i15] "a"(e.acc[1][13][3]));
  asm volatile("v_mul_f32 %[o0], %[i0], %[i1]\n\t" "v_mul_f32 %[o1], %[i2], %[i3]\n\t" "v_mul_f32 %[o2], %[i4], %[i5]\n\t" "v_mul_f32 %[o3], %[i6], %[i7]\n\t" "v_mul_f32 %[o4], %[i8], %[i9]\n\t" "v_mul_f32 %[o5], %[i10], %[i11]\n\t" "v_mul_f32 %[o6], %[i12], %[i13]\n\t" "v_mul_f32 %[o7], %[i14], %[i15]\n\t" : [o0] "=&v"(tz[0][0]), [o1] "=&v"(tz[1][0]), [o2] "=&v"(tz[0][1]), [o3] "=&v"(tz[1][1]), [o4] "=&v"(tz[0][2]), [o5] "=&v"(tz[1][2]), [o6] "=&v"(tz[0][3]), [o7] "=&v"(tz[1][3]) : [i0] "v"(gv[0][0]), [i1] "v"(sc.zr[0]), [i2] "v"(gv[1][0]), [i3] "v"(sc.zr[1]), [i4] "v"(gv[0][1]), [i5] "v"(sc.zr[0]), [i6] "v"(gv[1][1]), [i7] "v"(sc.zr[1]), [i8] "v"(gv[0][2]), [i9] "v"(sc.zr[0]), [i10] "v"(gv[1][2]), [i11] "v"(sc.zr[1]), [i12] "v"(gv[0][3]), [i13] "v"(sc.zr[0]), [i14] "v"(gv[1][3]), [i15] "v"(sc.zr[1]));
  asm volatile("v_exp_f32 %[o0], %[o0]\n\t" "v_exp_f32 %[o1], %[o1]\n\t" "v_exp_f32 %[o2], %[o2]\n\t" "v_exp_f32 %[o3], %[o3]\n\t" "v_exp_f32 %[o4], %[o4]\n\t" "v_exp_f32 %[o5], %[o5]\n\t" "v_exp_f32 %[o6], %[o6]\n\t" "v_exp_f32 %[o7], %[o7]\n\t" : [o0] "+v"(tz[0][0]), [o1] "+v"(tz[1][0]), [o2] "+v"(tz[0][1]), [o3] "+v"(tz[1][1]), [o4] "+v"(tz[0][2]), [o5] "+v"(tz[1][2]), [o6] "+v"(tz[0][3]), [o7] "+v"(tz[1][3]) : );
  asm volatile("v_fma_f32 %[o0], %[o0], %[i0], %[i0]\n\t" "v_fma_f32 %[o1], %[o1], %[i1], %[i1]\n\t" "v_fma_f32 %[o2], %[o2], %[i2], %[i2]\n\t" "v_fma_f32 %[o3], %[o3], %[i3], %[i3]\n\t" "v_fma_f32 %[o4], %[o4], %[i4], %[i4]\n\t" "v_fma_f32 %[o5], %[o5], %[i5], %[i5]\n\t" "v_fma_f32 %[o6], %[o6], %[i6], %[i6]\n\t" "v_fma_f32 %[o7], %[o7], %[i7], %[i7]\n\t" : [o0] "+v"(tz[0][0]), [o1] "+v"(tz[1][0]), [o2] "+v"(tz[0][1]), [o3] "+v"(tz[1][1]), [o4] "+v"(tz[0][2]), [o5] "+v"(tz[1][2]), [o6] "+v"(tz[0][3]), [o7] "+v"(tz[1][3]) : [i0] "v"(sc.ri[0]), [i1] "v"(sc.ri[1]), [i2] "v"(sc.ri[0]), [i3] "v"(sc.ri[1]), [i4] "v"(sc.ri[0]), [i5] "v"(sc.ri[1]), [i6] "v"(sc.ri[0]), [i7] "v"(sc.ri[1]));
  asm volatile("v_rcp_f32 %[o0], %[o0]\n\t" "v_rcp_f32 %[o1], %[o1]\n\t" "v_rcp_f32 %[o2], %[o2]\n\t" "v_rcp_f32 %[o3], %[o3]\n\t" "v_rcp_f32 %[o4], %[o4]\n\t" "v_rcp_f32 %[o5], %[o5]\n\t" "v_rcp_f32 %[o6], %[o6]\n\t" "v_rcp_f32 %[o7], %[o7]\n\t" : [o0] "+v"(tz[0][0]), [o1] "+v"(tz[1][0]), [o2] "+v"(tz[0][1]), [o3] "+v"(tz[1][1]), [o4] "+v"(tz[0][2]), [o5] "+v"(tz[1][2]), [o6] "+v"(tz[0][3]), [o7] "+v"(tz[1][3]) : );
  asm volatile("v_mul_f32 %[o0], %[i0], %[i1]\n\t" "v_mul_f32 %[o1], %[i2], %[i3]\n\t" "v_mul_f32 %[o2], %[i4], %[i5]\n\t" "v_mul_f32 %[o3], %[i6], %[i7]\n\t" "v_mul_f32 %[o4], %[i8], %[i9]\n\t" "v_mul_f32 %[o5], %[i10], %[i11]\n\t" "v_mul_f32 %[o6], %[i12], %[i13]\n\t" "v_mul_f32 %[o7], %[i14], %[i15]\n\t" : [o0] "=&v"(tw[0][0]), [o1] "=&v"(tw[1][0]), [o2] "=&v"(tw[0][1]), [o3] "=&v"(tw[1][1]), [o4] "=&v"(tw[0][2]), [o5] "=&v"(tw[1][2]), [o6] "=&v"(tw[0][3]), [o7] "=&v"(tw[1][3]) : [i0] "v"(uv[0][0]), [i1] "v"(tz[0][0]), [i2] "v"(uv[1][0]), [i3] "v"(tz[1][0]), [i4] "v"(uv[0][1]), [i5] "v"(tz[0][1]), [i6] "v"(uv[1][1]), [i7] "v"(tz[1][1]), [i8] "v"(uv[0][2]), [i9] "v"(tz[0][2]), [i10] "v"(uv[1][2]), [i11] "v"(tz[1][2]), [i12] "v"(uv[0][3]), [i13] "v"(tz[0][3]), [i14] "v"(uv[1][3]), [i15] "v"(tz[1][3]));
  asm volatile("v_cvt_pk_bf16_f32 %[o0], %[i0], %[i1]\n\t" "v_cvt_pk_bf16_f32 %[o1], %[i2], %[i3]\n\t" "v_cvt_pk_bf16_f32 %[o2], %[i4], %[i5]\n\t" "v_cvt_pk_bf16_f32 %[o3], %[i6], %[i7]\n\t" : [o0] "=&v"(pk[0][0][0]), [o1] "=&v"(pk[0][1][0]), [o2] "=&v"(pk[0][0][1]), [o3] "=&v"(pk[0][1][1]) : [i0] "v"(tw[0][0]), [i1] "v"(tw[0][1]), [i2] "v"(tw[1][0]), [i3] "v"(tw[1][1]), [i4] "v"(tw[0][2]), [i5] "v"(tw[0][3]), [i6] "v"(tw[1][2]), [i7] "v"(tw[1][3]));
  st128a(c.wsr, p0[0] + 0, so.pair[6], (u32x4){pk[1][0][0], pk[1][0][1], pk[0][0][0], pk[0][0][1]});
  st128a(c.wsr, p0[0] + 1024, so.pair[6], (u32x4){pk[1][1][0], pk[1][1][1], pk[0][1][0], pk[0][1][1]});
  st64a(c.wsr, p0[0] + 0, so.last, (u32x2){pk[0][0][0], pk[0][0][1]});
  st64a(c.wsr, p0[0] + 1024, so.last, (u32x2){pk[0][1][0], pk[0][1][1]});
  asm volatile("v_accvgpr_read_b32 %[o0], %[i0]\n\t" "v_accvgpr_read_b32 %[o1], %[i1]\n\t" "v_accvgpr_read_b32 %[o2], %[i2]\n\t" "v_accvgpr_read_b32 %[o3], %[i3]\n\t" "v_accvgpr_read_b32 %[o4], %[i4]\n\t" "v_accvgpr_read_b32 %[o5], %[i5]\n\t" "v_accvgpr_read_b32 %[o6], %[i6]\n\t" "v_accvgpr_read_b32 %[o7], %[i7]\n\t" "v_accvgpr_read_b32 %[o8], %[i8]\n\t" "v_accvgpr_read_b32 %[o9], %[i9]\n\t" "v_accvgpr_read_b32 %[o10], %[i10]\n\t" "v_accvgpr_read_b32 %[o11], %[i11]\n\t" "v_accvgpr_read_b32 %[o12], %[i12]\n\t" "v_accvgpr_read_b32 %[o13], %[i13]\n\t" "v_accvgpr_read_b32 %[o14], %[i14]\n\t" "v_accvgpr_read_b32 %[o15], %[i15]\n\t" : [o0] "=&v"(gv[0][0]), [o1] "=&v"(gv[1][0]), [o2] "=&v"(gv[0][1]), [o3] "=&v"(gv[1][1]), [o4] "=&v"(gv[0][2]), [o5] "=&v"(gv[1][2]), [o6] "=&v"(gv[0][3]), [o7] "=&v"(gv[1][3]), [o8] "=&v"(uv[0][0]), [o9] "=&v"(uv[1][0]), [o10] "=&v"(uv[0][1]), [o11] "=&v"(uv[1][1]), [o12] "=&v"(uv[0][2]), [o13] "=&v"(uv[1][2]), [o14] "=&v"(uv[0][3]), [o15] "=&v"(uv[1][3]) : [i0] "a"(e.acc[2][12][0]), [i1] "a"(e.acc[3][12][0]), [i2] "a"(e.acc[2][12][1]), [i3] "a"(e.acc[3][12][1]), [i4] "a"(e.acc[2][12][2]), [i5] "a"(e.acc[3][12][2]), [i6] "a"(e.acc[2][12][3]), [i7] "a"(e.acc[3][12][3]), [i8] "a"(e.acc[2][13][0]), [i9] "a"(e.acc[3][13][0]), [i10] "a"(e.acc[2][13][1]), [i11] "a"(e.acc[3][13][1]), [i12] "a"(e.acc[2][13][2]), [i13] "a"(e.acc[3][13][2]), [i14] "a"(e.acc[2][13][3]), [i15] "a"(e.acc[3][13][3]));
  asm volatile("v_mul_f32 %[o0], %[i0], %[i1]\n\t" "v_mul_f32 %[o1], %[i2], %[i3]\n\t" "v_mul_f32 %[o2], %[i4], %[i5]\n\t" "v_mul_f32 %[o3], %[i6], %[i7]\n\t" "v_mul_f32 %[o4], %[i8], %[i9]\n\t" "v_mul_f32 %[o5], %[i10], %[i11]\n\t" "v_mul_f32 %[o6], %[i12], %[i13]\n\t" "v_mul_f32 %[o7], %[i14], %[i15]\n\t" : [o0] "=&v"(tz[0][0]), [o1] "=&v"(tz[1][0]), [o2] "=&v"(tz[0][1]), [o3] "=&v"(tz[1][1]), [o4] "=&v"(tz[0][2]), [o5] "=&v"(tz[1][2]), [o6] "=&v"(tz[0][3]), [o7] "=&v"(tz[1][3]) : [i0] "v"(gv[0][0]), [i1] "v"(sc.zr[2]), [i2] "v"(gv[1][0]), [i3] "v"(sc.zr[3]), [i4] "v"(gv[0][1]), [i5] "v"(sc.zr[2]), [i6] "v"(gv[1][1]), [i7] "v"(sc.zr[3]), [i8] "v"(gv[0][2]), [i9] "v"(sc.zr[2]), [i10] "v"(gv[1][2]), [i11] "v"(sc.zr[3]), [i12] "v"(gv[0][3]), [i13] "v"(sc.zr[2]), [i14] "v"(gv[1][3]), [i15] "v"(sc.zr[3]));
  asm volatile("v_exp_f32 %[o0], %[o0]\n\t" "v_exp_f32 %[o1], %[o1]\n\t" "v_exp_f32 %[o2], %[o2]\n\t" "v_exp_f32 %[o3], %[o3]\n\t" "v_exp_f32 %[o4], %[o4]\n\t" "v_exp_f32 %[o5], %[o5]\n\t" "v_exp_f32 %[o6], %[o6]\n\t" "v_exp_f32 %[o7], %[o7]\n\t" : [o0] "+v"(tz[0][0]), [o1] "+v"(tz[1][0]), [o2] "+v"(tz[0][1]), [o3] "+v"(tz[1][1]), [o4] "+v"(tz[0][2]), [o5] "+v"(tz[1][2]), [o6] "+v"(tz[0][3]), [o7] "+v"(tz[1][3]) : );
  asm volatile("v_fma_f32 %[o0], %[o0], %[i0], %[i0]\n\t" "v_fma_f32 %[o1], %[o1], %[i1], %[i1]\n\t" "v_fma_f32 %[o2], %[o2], %[i2], %[i2]\n\t" "v_fma_f32 %[o3], %[o3], %[i3], %[i3]\n\t" "v_fma_f32 %[o4], %[o4], %[i4], %[i4]\n\t" "v_fma_f32 %[o5], %[o5], %[i5], %[i5]\n\t" "v_fma_f32 %[o6], %[o6], %[i6], %[i6]\n\t" "v_fma_f32 %[o7], %[o7], %[i7], %[i7]\n\t" : [o0] "+v"(tz[0][0]), [o1] "+v"(tz[1][0]), [o2] "+v"(tz[0][1]), [o3] "+v"(tz[1][1]), [o4] "+v"(tz[0][2]), [o5] "+v"(tz[1][2]), [o6] "+v"(tz[0][3]), [o7] "+v"(tz[1][3]) : [i0] "v"(sc.ri[2]), [i1] "v"(sc.ri[3]), [i2] "v"(sc.ri[2]), [i3] "v"(sc.ri[3]), [i4] "v"(sc.ri[2]), [i5] "v"(sc.ri[3]), [i6] "v"(sc.ri[2]), [i7] "v"(sc.ri[3]));
  asm volatile("v_rcp_f32 %[o0], %[o0]\n\t" "v_rcp_f32 %[o1], %[o1]\n\t" "v_rcp_f32 %[o2], %[o2]\n\t" "v_rcp_f32 %[o3], %[o3]\n\t" "v_rcp_f32 %[o4], %[o4]\n\t" "v_rcp_f32 %[o5], %[o5]\n\t" "v_rcp_f32 %[o6], %[o6]\n\t" "v_rcp_f32 %[o7], %[o7]\n\t" : [o0] "+v"(tz[0][0]), [o1] "+v"(tz[1][0]), [o2] "+v"(tz[0][1]), [o3] "+v"(tz[1][1]), [o4] "+v"(tz[0][2]), [o5] "+v"(tz[1][2]), [o6] "+v"(tz[0][3]), [o7] "+v"(tz[1][3]) : );
  asm volatile("v_mul_f32 %[o0], %[i0], %[i1]\n\t" "v_mul_f32 %[o1], %[i2], %[i3]\n\t" "v_mul_f32 %[o2], %[i4], %[i5]\n\t" "v_mul_f32 %[o3], %[i6], %[i7]\n\t" "v_mul_f32 %[o4], %[i8], %[i9]\n\t" "v_mul_f32 %[o5], %[i10], %[i11]\n\t" "v_mul_f32 %[o6], %[i12], %[i13]\n\t" "v_mul_f32 %[o7], %[i14], %[i15]\n\t" : [o0] "=&v"(tw[0][0]), [o1] "=&v"(tw[1][0]), [o2] "=&v"(tw[0][1]), [o3] "=&v"(tw[1][1]), [o4] "=&v"(tw[0][2]), [o5] "=&v"(tw[1][2]), [o6] "=&v"(tw[0][3]), [o7] "=&v"(tw[1][3]) : [i0] "v"(uv[0][0]), [i1] "v"(tz[0][0]), [i2] "v"(uv[1][0]), [i3] "v"(tz[1][0]), [i4] "v"(uv[0][1]), [i5] "v"(tz[0][1]), [i6] "v"(uv[1][1]), [i7] "v"(tz[1][1]), [i8] "v"(uv[0][2]), [i9] "v"(tz[0][2]), [i10] "v"(uv[1][2]), [i11] "v"(tz[1][2]), [i12] "v"(uv[0][3]), [i13] "v"(tz[0][3]), [i14] "v"(uv[1][3]), [i15] "v"(tz[1][3]));
  asm volatile("v_cvt_pk_bf16_f32 %[o0], %[i0], %[i1]\n\t" "v_cvt_pk_bf16_f32 %[o1], %[i2], %[i3]\n\t" "v_cvt_pk_bf16_f32 %[o2], %[i4], %[i5]\n\t" "v_cvt_pk_bf16_f32 %[o3], %[i6], %[i7]\n\t" : [o0] "=&v"(pk[0][2][0]), [o1] "=&v"(pk[0][3][0]), [o2] "=&v"(pk[0][2][1]), [o3] "=&v"(pk[0][3][1]) : [i0] "v"(tw[0][0]), [i1] "v"(tw[0][1]), [i2] "v"(tw[1][0]), [i3] "v"(tw[1][1]), [i4] "v"(tw[0][2]), [i5] "v"(tw[0][3]), [i6] "v"(tw[1][2]), [i7] "v"(tw[1][3]));
  st128a(c.wsr, p0[1] + 0, so.pair[6], (u32x4){pk[1][2][0], pk[1][2][1], pk[0][2][0], pk[0][2][1]});
  st128a(c.wsr, p0[1] + 1024, so.pair[6], (u32x4){pk[1][3][0], pk[1][3][1], pk[0][3][0], pk[0][3][1]});
  st64a(c.wsr, p0[1] + 0, so.last, (u32x2){pk[0][2][0], pk[0][2][1]});
  st64a(c.wsr, p0[1] + 1024, so.last, (u32x2){pk[0][3][0], pk[0][3][1]});
}
DI void phase4(const Ctx& c) {
  const u32 lane = fresh_lane(), w = c.w, rb = c.bi & 7, j = c.xcd * 4 + (c.bi >> 3);
  const u32 G64 = rb * 4 + w, g = lane >> 4;
  EngG e;
#pragma unroll
  for (int i = 0; i < 2; ++i) {
    const u32 row = 8 * w + 4 * i + (lane >> 4), c16 = lane & 15;
    u32 nb = (c16 >> 1) ^ (row & 7); nb = nb > 6 ? 6 : nb;
    e.wvoff[i] = (row * FF + j * 112 + nb * 16 + (c16 & 1) * 8) * 2;
  }
  e.tvoff = lane * 16;
  e.wsrd[0] = mk_rsrc(c.wg, D * FF * 2); e.wsrd[1] = mk_rsrc(c.wu, D * FF * 2);
  e.tsrd = mk_rsrc(c.ws + WS_HC + (size_t)G64 * (32 * 4096), 32 * 4096);
  e.m0base = c.ldsbase + w * 2048;
  asm volatile("s_nop 4");
  g_pro_w(e, std::make_integer_sequence<int, 28>{});
  e.wsoff = 7 * G_WSTEP;
  { const u32 q = (lane >> 2) & 3, p = lane & 3, row = 4 * g + q, f = row & 7;
#pragma unroll
    for (int b = 0; b < 2; ++b)
#pragma unroll
      for (int n = 0; n < 7; ++n) e.laddr[b][n] = c.ldsbase + b * 4 * G_STAGE + row * 256 + ((n ^ f) * 32) + p * 8; }
  await(c, FL_H, lane < 32 ? rb * 32 + lane : 0xffffffffu);
  u32x4 st[4][2];
  { const u32 vo = WS_ST + G64 * 4 * 2048 + lane * 16;
    asm volatile("s_nop 4");
#pragma unroll
    for (int tb = 0; tb < 4; ++tb) { ld128<0>(st[tb][0], vo, c.wsr, tb * 2048); ld128<1024>(st[tb][1], vo, c.wsr, tb * 2048); } }
  g_tok<0, 0>(e, 0); g_tok<0, 1>(e, 0); g_tok<0, 2>(e, 0); g_tok<0, 3>(e, 0);
  g_tok<1, 0>(e, 4096); g_tok<1, 1>(e, 4096); g_tok<1, 2>(e, 4096); g_tok<1, 3>(e, 4096);
  g_tok<2, 0>(e, 8192); g_tok<2, 1>(e, 8192); g_tok<2, 2>(e, 8192); g_tok<2, 3>(e, 8192);
  e.tsoff = 7 * 4096;
#pragma unroll
  for (int t = 0; t < 4; ++t)
#pragma unroll
    for (int n = 0; n < 14; ++n) {
      e.acc[t][n] = (f32x4){0.f, 0.f, 0.f, 0.f};
      if (n >= 10) asm volatile("" : "+a"(e.acc[t][n])); else PINV(e.acc[t][n]);
    }
  wait_vm<12>();
  GluSc sc;
#pragma unroll
  for (int tb = 0; tb < 4; ++tb) {
    PINV(st[tb][0]); PINV(st[tb][1]);
    const f32x4 a = __builtin_bit_cast(f32x4, st[tb][0]), b = __builtin_bit_cast(f32x4, st[tb][1]);
    const float ms = gsum((a[0] + a[2]) + (b[0] + b[2])) * (1.f / D);
    const float var = gsum((a[1] + a[3]) + (b[1] + b[3])) * (1.f / D) - ms * ms + LN_EPS;
    sc.zr[tb] = -LOG2E * __builtin_amdgcn_rsqf(var); sc.ri[tb] = __builtin_amdgcn_sqrtf(var);
    PINV(sc.zr[tb]); PINV(sc.ri[tb]);
  }
  asm volatile("s_waitcnt vmcnt(4) lgkmcnt(0)\n\ts_barrier" ::: "memory");
  g_rw<0, 0, 0, 0>(e); g_rw<0, 0, 0, 1>(e); g_rw<0, 1, 1, 0>(e); g_rw<0, 1, 1, 1>(e); g_rw<0, 2, 2, 0>(e); g_rw<0, 2, 2, 1>(e);
  g_ring<1>(e, std::make_integer_sequence<int, 8>{});
#pragma unroll 1
  for (int it = 0; it < 2; ++it) g_ring<0>(e, std::make_integer_sequence<int, 8>{});
  g_stage<2, 0>(e, std::make_integer_sequence<int, 14>{});
  asm volatile("s_waitcnt vmcnt(8)\n\ts_barrier" ::: "memory");
  asm volatile("ds_read_b64_tr_b16 %0, %1 offset:16384" : "=v"(e.wlo[0]) : "v"(e.laddr[0][0]));
  asm volatile("ds_read_b64_tr_b16 %0, %1 offset:20480" : "=v"(e.whi[0]) : "v"(e.laddr[0][0]));
  asm volatile("ds_read_b64_tr_b16 %0, %1 offset:24576" : "=v"(e.wlo[1]) : "v"(e.laddr[0][0]));
  asm volatile("ds_read_b64_tr_b16 %0, %1 offset:28672" : "=v"(e.whi[1]) : "v"(e.laddr[0][0]));
  asm volatile("ds_read_b64_tr_b16 %0, %1 offset:32768" : "=v"(e.wlo[2]) : "v"(e.laddr[0][0]));
  asm volatile("ds_read_b64_tr_b16 %0, %1 offset:36864" : "=v"(e.whi[2]) : "v"(e.laddr[0][0]));
  { const u32 lane2 = fresh_lane(), odd = j & 1, OOB = 0x40000000u;
    const u32 pb = WS_ACT + (G64 * 2 * 112 * 2) * 1024 + lane2 * 16 + ((j * 7) >> 1) * 2048;
    const u32 p0[2] = {pb, pb + 224 * 1024};
    GluSo so;
    so.first = odd ? 8u : OOB; so.last = odd ? OOB : 3u * 2048u; so.pair[0] = OOB;
#pragma unroll
    for (u32 n = 1; n < 7; ++n) so.pair[n] = ((n + odd) & 1) ? ((n + odd) >> 1) * 2048u : OOB;
    p4_tail(c, e, sc, p0, so); }
  publish(c, FL_ACT, rb * 32 + j);
}

struct Args { const u16 *x, *wqkv, *wo, *wg, *wu, *wd; char* ws; u16* out; };
__global__ __launch_bounds__(256, 1) void fused_block(Args a) {
  extern __shared__ __attribute__((aligned(16))) char lds_[];
  LAS char* lds = (LAS char*)lds_;
  Ctx c;
  c.x = a.x; c.wqkv = a.wqkv; c.wo = a.wo; c.wg = a.wg; c.wu = a.wu; c.wd = a.wd; c.ws = a.ws; c.out = a.out;
  c.w = rfl(threadIdx.x >> 6);
  c.ldsbase = (u32)(size_t)lds; c.xcd = blockIdx.x & 7; c.bi = blockIdx.x >> 3;
  c.wsr = mk_rsrc(a.ws, (u32)WS_END);
  {
    u64 pc; u32 len;
    asm volatile("s_getpc_b64 %0\nfused_code_mark:\n\ts_mov_b32 %1, fused_code_end-fused_code_mark" : "=s"(pc), "=s"(len));
    const rsrc_t cr = mk_rsrc((const void*)pc, len & ~15u);
    if (c.w == 3) {
      const u32 vo = c.bi * 4096 + fresh_lane() * 16;
      asm volatile("s_mov_b32 m0, %0\n\ts_nop 4\n\tbuffer_load_dwordx4 %1, %2, 0 offen lds\n\tbuffer_load_dwordx4 %1, %2, 0 offen offset:1024 lds\n\t"
                   "buffer_load_dwordx4 %1, %2, 0 offen offset:2048 lds\n\tbuffer_load_dwordx4 %1, %2, 0 offen offset:3072 lds"
                   ::"s"(c.ldsbase + 155648), "v"(vo), "s"(cr) : "memory");
    } }
  const u64 nonce = __hip_atomic_load((u64*)(a.ws + WS_FL), __ATOMIC_RELAXED, __HIP_MEMORY_SCOPE_AGENT);
  c.tag = 0;
  phase01(c, lds, nonce);
  phase2(c, lds);
  phase3(c, lds);
  phase4(c);
  phase5(c, lds);
  if (blockIdx.x == 0 && c.w == 0 && fresh_lane() == 0) __hip_atomic_store((u64*)(a.ws + WS_FL), c.tag, __ATOMIC_RELAXED, __HIP_MEMORY_SCOPE_AGENT);
  asm volatile("fused_code_end:");
}

extern "C" void kernel_launch(void* const* d_in, const int* in_sizes, int n_in, void* d_out, int out_size, void* d_ws, size_t ws_size, hipStream_t stream) {
  Args a;
  a.x = (const u16*)d_in[0]; a.wqkv = (const u16*)d_in[1]; a.wo = (const u16*)d_in[2];
  a.wg = (const u16*)d_in[4]; a.wu = (const u16*)d_in[5]; a.wd = (const u16*)d_in[6];
  a.ws = (char*)d_ws; a.out = (u16*)d_out;
  (void)hipFuncSetAttribute((const void*)fused_block, hipFuncAttributeMaxDynamicSharedMemorySize, LDS_BYTES);
  fused_block<<<256, 256, LDS_BYTES, stream>>>(a);
}
```
